```python
import torch

def _softmax(x, axis=-1):
    return torch.softmax(x, dim=axis)
def _gelu(x):
    return torch.nn.functional.gelu(x, approximate="tanh")
def _silu(x):
    return torch.nn.functional.silu(x)
def _sigmoid(x):
    return torch.sigmoid(x)
def _layernorm(x, axis=-1, eps=1e-5):
    m = x.mean(dim=axis, keepdim=True)
    v = x.var(dim=axis, keepdim=True, unbiased=False)
    return (x - m) / torch.sqrt(v + eps)
def _scatter_add(tgt, idx, src, axis=0):
    return tgt.clone().index_add_(axis, idx, src)
def _segment_sum(src, seg, n):
    out = torch.zeros((n,) + src.shape[1:], dtype=src.dtype)
    return out.index_add_(0, seg, src)
def _pad(x, pw):
    flat = tuple(v for pair in reversed(pw) for v in pair)
    return torch.nn.functional.pad(x, flat)

kernel_name = 'gated_mix__linear_attn_block__glu_gated_swiglu__prompt'

def setup_inputs():

    _w = (lambda a: (lambda t: t.to(torch.bfloat16) if t.is_floating_point() else t)(torch.as_tensor(a)))
    def _A_setup():
        B, S, D = 1, 2048, 448
        return {
            "x": _w((lambda *s: torch.randn(*s, dtype=torch.bfloat16) * 0.05)(B, S, D)),
            "wqkv": _w((lambda *s: torch.randn(*s, dtype=torch.bfloat16) * 0.8)(D, 3 * D)),
            "wo": _w((lambda *s: torch.randn(*s, dtype=torch.bfloat16) * 0.05)(D, D)),
        }
    _A_in = _A_setup()

    def _B_setup():
        B, S, D, F = 1, 2048, 448, 1792
        return {
            "x": _w((lambda *s: torch.randn(*s, dtype=torch.bfloat16) * 0.05)(B, S, D)),
            "w1": _w((lambda *s: torch.randn(*s, dtype=torch.bfloat16) * 1.6)(D, F) * (2.0 / D) ** 0.5),
            "w3": _w((lambda *s: torch.randn(*s, dtype=torch.bfloat16) * 1.6)(D, F) * (2.0 / D) ** 0.5),
            "w2": _w((lambda *s: torch.randn(*s, dtype=torch.bfloat16) * 1.6)(F, D) * (2.0 / F) ** 0.5),
        }
    _B_in = _B_setup()

    return {
        "A_x": _A_in["x"],
        "A_wqkv": _A_in["wqkv"],
        "A_wo": _A_in["wo"],
        "B_x": _B_in["x"],
        "B_w1": _B_in["w1"],
        "B_w3": _B_in["w3"],
        "B_w2": _B_in["w2"],
        "gate_w": _w((lambda *s: torch.randn(*s, dtype=torch.bfloat16) * 0.05)(448) * 16.0),
    }

def reference(A_x, A_wqkv, A_wo, B_x, B_w1, B_w3, B_w2, gate_w):
    def _A_ref(x, wqkv, wo):
        B, S, D = x.shape
        H = 16; Dh = D // H
        qkv = (lambda x, *d: x.permute(*d))((x @ wqkv).reshape(B, S, 3, H, Dh), 2, 0, 3, 1, 4)
        q, k, v = qkv[0], qkv[1], qkv[2]
        phi = lambda t: torch.where(t > 0, t + 1, torch.exp(t)) + 1e-6
        pq, pk = phi(q), phi(k)
        kv = torch.einsum("bhsd,bhse->bhde", pk, v)
        z = torch.einsum("bhsd->bhd", pk)
        num = torch.einsum("bhqd,bhde->bhqe", pq, kv)
        den = torch.einsum("bhqd,bhd->bhq", pq, z)[..., None] + 1e-6
        o = num / den
        return (lambda x, *d: x.permute(*d))(o, 0, 2, 1, 3).reshape(B, S, D) @ wo

    def _B_ref(x, w1, w3, w2):
        gate = torch.einsum("bsd,df->bsf", x, w1)
        up = torch.einsum("bsd,df->bsf", x, w3)
        h = _silu(gate) * up
        out = torch.einsum("bsf,fd->bsd", h, w2)
        return out

    x = A_x
    g = _sigmoid((x * gate_w).sum(-1, keepdims=True))
    a = _A_ref(x, A_wqkv, A_wo)
    b = _B_ref(x, B_w1, B_w3, B_w2)
    return g * a + (1 - g) * b
```

```cpp
#include <hip/hip_runtime.h>
#include <hip/hip_bf16.h>
#include <stdint.h>

typedef __attribute__((ext_vector_type(8))) short bf16x8;
typedef __attribute__((ext_vector_type(4))) short s16x4;
typedef __attribute__((ext_vector_type(8))) __bf16 bf16x8b;
typedef __attribute__((ext_vector_type(4))) __bf16 bf16x4b;
typedef __attribute__((ext_vector_type(8))) float f32x8;
typedef __attribute__((ext_vector_type(4))) float f32x4;
typedef __attribute__((ext_vector_type(16))) float f32x16;
typedef __attribute__((ext_vector_type(4))) unsigned u32x4;
typedef __attribute__((ext_vector_type(2))) unsigned u32x2;
typedef __attribute__((address_space(3))) s16x4* lds_s16x4_ptr;
typedef __attribute__((address_space(3))) unsigned* lds_u32_ptr;
typedef __attribute__((address_space(1))) unsigned gu32;

#define DI __device__ __forceinline__
#define MFMA32(a, b, c) __builtin_amdgcn_mfma_f32_32x32x16_bf16((a), (b), (c), 0, 0, 0)

constexpr int NKS = 144;
constexpr unsigned WS_H = 0;
constexpr unsigned WS_W = WS_H + 64u * NKS * 1024u;
constexpr unsigned WS_KV = WS_W + 14u * NKS * 1024u;
constexpr unsigned WS_Z = WS_KV + 16u * 16u * 4096u;
constexpr unsigned WS_G = WS_Z + 1024u;
constexpr unsigned WS_CNT = WS_G + 8192u;
constexpr unsigned WS_END = WS_CNT + 256u;

DI bf16x8 pack8(f32x8 v) { return __builtin_bit_cast(bf16x8, __builtin_convertvector(v, bf16x8b)); }
DI float bf2f(unsigned short u) { return __uint_as_float(((unsigned)u) << 16); }
DI float phi_f(float t) { return (t > 0.f ? t + 1.f : __expf(t)) + 1e-6f; }
DI void glds16(const char* sbase, unsigned voff, unsigned lds_dst) {
  unsigned keep;
  asm volatile("s_mov_b32 %0, m0\n\ts_mov_b32 m0, %3\n\ts_nop 0\n\tglobal_load_lds_dwordx4 %1, %2\n\ts_mov_b32 m0, %0"
               : "=&s"(keep) : "v"(voff), "s"(sbase), "s"(lds_dst) : "memory");
}

constexpr int K1_XOFF = 98304;
constexpr int K1_LDS = K1_XOFF + 3 * 16384;

#define K1_WAITBAR(n) asm volatile("s_waitcnt vmcnt(" #n ") lgkmcnt(0)\n\ts_barrier" ::: "memory")

template <int NS, int NT, int KIND, int NLC>
DI void k1_main(char* smem, int w, const char* const (&wsrc)[NLC / 2], const unsigned (&wvo)[NLC / 2],
                const unsigned (&wstr)[NLC / 2], const char* xsrc, unsigned xvo,
                const unsigned (&taddr)[NT], unsigned xaddr0, f32x16 (&acc)[NS][NT]) {
  constexpr int WI = NLC / 2;
  constexpr int R = (NLC == 6) ? 4 : 3;
  constexpr int WSLOT = NLC * 4096;
  const unsigned lds0 = (unsigned)(uintptr_t)(__attribute__((address_space(3))) char*)smem;
  const unsigned wdst0 = lds0 + (unsigned)((w >> 2) * 4096 + (w & 3) * 1024);
  const unsigned xdst0 = lds0 + (unsigned)(K1_XOFF + w * 1024);

  auto issueW = [&](int st, int slot) {
#pragma unroll
    for (int m = 0; m < WI; ++m)
      glds16(wsrc[m] + (size_t)((unsigned)st * wstr[m]), wvo[m], (unsigned)(slot * WSLOT) + wdst0 + m * 8192);
  };
  auto issueX = [&](int m, int xslot) {
    glds16(xsrc + (size_t)((unsigned)(m & 1) * (64u * 896u) + (unsigned)(m >> 1) * 128u), xvo,
           xdst0 + (unsigned)(xslot * 16384 + (m & 1) * 8192));
  };

  if constexpr (R == 4) {
    issueX(0, 0); issueW(0, 0); issueX(1, 0); issueW(1, 1); issueX(2, 1); issueW(2, 2); issueX(3, 1);
  } else {
    issueX(0, 0); issueX(1, 0); issueW(0, 0); issueX(2, 1); issueW(1, 1); issueX(3, 1);
  }

  int slot = 0;
  int pslot = R - 1;
  int xslot = 0;
  int pxslot = 2;
#pragma unroll 1
  for (int s = 0; s < 14; ++s) {
    if constexpr (R == 4) {
      if (s <= 10) K1_WAITBAR(8); else if (s == 11) K1_WAITBAR(7); else if (s == 12) K1_WAITBAR(3); else K1_WAITBAR(0);
    } else {
      if (s <= 10) K1_WAITBAR(6); else if (s == 11) K1_WAITBAR(5); else if (s == 12) K1_WAITBAR(4); else K1_WAITBAR(0);
    }
    if (s + R - 1 < 14) issueW(s + R - 1, pslot);
    if (s + 4 < 14) issueX(s + 4, pxslot);

    const char* wb = smem + slot * WSLOT;
    const char* xb = smem + K1_XOFF + xslot * 16384;
    const unsigned kx = (unsigned)((s & 1) << 6);
#pragma unroll
    for (int u = 0; u < 2; ++u) {
      bf16x8 xf[NS], wf[NT];
#pragma unroll
      for (int st = 0; st < NS; ++st) xf[st] = *(const bf16x8*)(xb + ((xaddr0 + st * 4096) ^ (kx + u * 32)));
#pragma unroll
      for (int t = 0; t < NT; ++t) {
        s16x4 lo = __builtin_amdgcn_ds_read_tr16_b64_v4i16((lds_s16x4_ptr)(wb + taddr[t] + u * 2048));
        s16x4 hi = __builtin_amdgcn_ds_read_tr16_b64_v4i16((lds_s16x4_ptr)(wb + taddr[t] + u * 2048 + 512));
        wf[t] = __builtin_shufflevector(lo, hi, 0, 1, 2, 3, 4, 5, 6, 7);
      }
#pragma unroll
      for (int t = 0; t < NT; ++t)
#pragma unroll
        for (int st = 0; st < NS; ++st) {
          if constexpr (KIND == 0) acc[st][t] = MFMA32(wf[t], xf[st], acc[st][t]);
          else acc[st][t] = MFMA32(xf[st], wf[t], acc[st][t]);
        }
    }
    slot = (slot + 1 == R) ? 0 : slot + 1;
    pslot = (pslot + 1 == R) ? 0 : pslot + 1;
    if (s & 1) { xslot = (xslot == 2) ? 0 : xslot + 1; pxslot = (pxslot == 2) ? 0 : pxslot + 1; }
  }
}

DI unsigned taddr_ffn(int lane, int i) {
  const int hh = lane >> 5, blk = (lane >> 4) & 1, q = (lane & 15) >> 2, p = lane & 3;
  const int lc = (i >> 2) + 2 * blk, inoff = (i & 3) * 32 + 8 * p;
  const int pos = (inoff >> 4) ^ (4 * ((q >> 1) & 1)) ^ (2 * blk);
  return (unsigned)(lc * 4096 + (8 * hh + q) * 128 + pos * 16 + (inoff & 15));
}
DI unsigned taddr_head(int lane, int lcb, int o) {
  const int hh = lane >> 5, blk = (lane >> 4) & 1, q = (lane & 15) >> 2, p = lane & 3;
  const int pp = (blk == 1 && p == 3) ? 2 : p;
  const int vo = o + 32 * blk + 8 * pp;
  const int lc = lcb + (vo >> 7), inoff = vo & 127;
  const int pos = (inoff >> 4) ^ (4 * ((q >> 1) & 1));
  return (unsigned)(lc * 4096 + (8 * hh + q) * 128 + pos * 16 + (inoff & 15));
}

template <int NS, int NT>
DI void k1_epi_ffn(f32x16 (&acc)[NS][NT], char* ws, int lane, int stile_g, int kstep0) {
#pragma unroll
  for (int st = 0; st < NS; ++st)
#pragma unroll
    for (int t = 0; t < NT; ++t) {
      f32x8 hv;
#pragma unroll
      for (int j = 0; j < 8; ++j) {
        const float gt = acc[st][t][j], up = acc[st][t][j + 8];
        hv[j] = gt * __builtin_amdgcn_rcpf(1.f + __expf(-gt)) * up;
      }
      *(bf16x8*)(ws + WS_H + (unsigned)((stile_g + st) * NKS + kstep0 + t) * 1024u + lane * 16) = pack8(hv);
    }
}

template <int NS, int NT>
DI void k1_epi_q(f32x16 (&acc)[NS][NT], char* ws, int lane, int stile_g, int head0) {
  const bool hi = lane >= 32;
#pragma unroll
  for (int st = 0; st < NS; ++st)
#pragma unroll
    for (int t = 0; t < NT; ++t) {
      f32x8 a, b;
#pragma unroll
      for (int j = 0; j < 8; ++j) {
        a[j] = phi_f(acc[st][t][j]);
        const float pb = phi_f(acc[st][t][j + 8]);
        b[j] = (j >= 4 && hi) ? 0.f : pb;
      }
      char* dst = ws + WS_H + (unsigned)((stile_g + st) * NKS + 112 + 2 * (head0 + t)) * 1024u + lane * 16;
      *(bf16x8*)dst = pack8(a);
      *(bf16x8*)(dst + 1024) = pack8(b);
    }
}

template <int NS, int NT>
DI void k1_epi_kv(f32x16 (&acc)[NS][NT], char* smem, int lane, int slot0, int slot_stride) {
  const int r = lane & 31;
#pragma unroll
  for (int pi = 0; pi < NT / 2; ++pi) {
    f32x16 kv = {};
#pragma unroll
    for (int st = 0; st < NS; ++st)
#pragma unroll
      for (int t = 0; t < 2; ++t) {
        f32x8 pk, vv;
#pragma unroll
        for (int j = 0; j < 8; ++j) {
          const float kk = phi_f(acc[st][2 * pi][8 * t + j]);
          pk[j] = (r < 28) ? kk : 0.f;
          const float v0 = acc[st][2 * pi + 1][8 * t + j];
          vv[j] = (r < 28) ? v0 : ((r == 28) ? 1.f : 0.f);
        }
        kv = MFMA32(pack8(pk), pack8(vv), kv);
      }
    char* dst = smem + (slot0 + pi * slot_stride) * 4096 + lane * 16;
#pragma unroll
    for (int g = 0; g < 4; ++g) {
      f32x4 v = {kv[4 * g], kv[4 * g + 1], kv[4 * g + 2], kv[4 * g + 3]};
      *(f32x4*)(dst + g * 1024) = v;
    }
  }
}

__global__ __launch_bounds__(512, 2) void gm_k1(const unsigned short* __restrict__ x, const unsigned short* __restrict__ wqkv,
                                                const unsigned short* __restrict__ w1, const unsigned short* __restrict__ w3,
                                                const unsigned short* __restrict__ w2, const unsigned short* __restrict__ gw,
                                                char* __restrict__ ws) {
  extern __shared__ __attribute__((aligned(16))) char smem[];
  const int tid = threadIdx.x, lane = tid & 63;
  const int w = __builtin_amdgcn_readfirstlane(tid >> 6);
  const int b = blockIdx.x, xcd = b & 7, bi = b >> 3;
  const int rg = 8 * (xcd & 1) + (bi & 7);
  const int cq = 4 * (xcd >> 1) + (bi >> 3);
  const int cg = (cq < 8) ? (((cq & 3) == 3) ? 14 + (cq >> 2) : 3 * (cq >> 2) + (cq & 3)) : cq - 2;
  const int type = (cg < 8) ? 0 : ((cg < 14) ? 1 : 2);

  const int gwv = b * 8 + w;
  const int rt_t = gwv / 14, rt_n = gwv - rt_t * 14;
  unsigned short w2v[8];
  {
    const int tt = (gwv < 1568) ? rt_t : 0;
    const unsigned short* src = w2 + (size_t)(16 * tt + 4 * (lane >> 5)) * 448 + 32 * rt_n + (lane & 31);
#pragma unroll
    for (int j = 0; j < 8; ++j) w2v[j] = src[(8 * (j >> 2) + (j & 3)) * 448];
  }
  u32x4 gx, gg;
  {
    const int l2 = lane < 56 ? lane : 55;
    gx = *(const u32x4*)(x + (size_t)gwv * 448 + l2 * 8);
    gg = *(const u32x4*)(gw + l2 * 8);
  }
  if (b == 0 && tid == 0) *(unsigned*)(ws + WS_CNT) = 0u;

  const int drow = 8 * (w & 3) + (lane >> 3);
  const int dpos = lane & 7;
  const int dsw = 4 * ((lane >> 4) & 1);
  const char* xsrc = (const char*)x + (size_t)(rg * 128) * 896;
  unsigned xvo;
  {
    const int row = 8 * w + (lane >> 3);
    const int c = dpos ^ ((row >> 1) & 7);
    xvo = (unsigned)(row * 896 + c * 16);
  }
  const int hh = lane >> 5, r = lane & 31;
  const int wh = w >> 2;

  if (type != 2) {
    const char* wsrc[3];
    unsigned wstr[3], wvo[3];
    const int u = cg;
    wsrc[0] = (const char*)w1 + 256 * u + 128 * wh;
    wsrc[1] = (const char*)w3 + 256 * u + 128 * wh;
    wvo[0] = (unsigned)(drow * 3584 + ((dpos ^ dsw) << 4));
    wvo[1] = (unsigned)(drow * 3584 + ((dpos ^ dsw ^ 2) << 4));
    wvo[2] = (unsigned)(drow * 2688 + ((dpos ^ dsw) << 4));
    wstr[0] = wstr[1] = 32u * 3584u; wstr[2] = 32u * 2688u;
    int hd = 0, o0 = 0, o1 = 0, lcb1 = 4;
    if (type == 0) {
      const int byte0 = 112 * cg, L0 = byte0 >> 7;
      const int L1 = ((byte0 + 111) >> 7);
      wsrc[2] = (const char*)wqkv + (wh ? L1 : L0) * 128;
      o0 = byte0 - L0 * 128; o1 = o0 + 56; lcb1 = 4;
    } else {
      const int i6 = cg - 8;
      hd = (i6 < 2) ? i6 : ((i6 < 4) ? i6 + 5 : i6 + 10);
      const int byte0 = 56 * hd, L0 = byte0 >> 7;
      wsrc[2] = (const char*)wqkv + (wh ? 1792 : 896) + L0 * 128;
      o0 = byte0 - L0 * 128; o1 = o0; lcb1 = 5;
    }
    const int sh = w & 1, qt = w >> 1;
    const unsigned xaddr0 = (unsigned)((sh * 64 + r) * 128 + ((hh ^ ((r >> 1) & 7)) << 4));
    const int stile_g = rg * 4 + sh * 2;
    if (qt < 2) {
      unsigned ta[3];
#pragma unroll
      for (int t = 0; t < 3; ++t) ta[t] = taddr_ffn(lane, 3 * qt + t);
      f32x16 acc[2][3] = {};
      k1_main<2, 3, 0, 6>(smem, w, wsrc, wvo, wstr, xsrc, xvo, ta, xaddr0, acc);
      k1_epi_ffn<2, 3>(acc, ws, lane, stile_g, 8 * u + 3 * qt);
    } else if (qt == 2) {
      unsigned ta[2];
#pragma unroll
      for (int t = 0; t < 2; ++t) ta[t] = taddr_ffn(lane, 6 + t);
      f32x16 acc[2][2] = {};
      k1_main<2, 2, 0, 6>(smem, w, wsrc, wvo, wstr, xsrc, xvo, ta, xaddr0, acc);
      k1_epi_ffn<2, 2>(acc, ws, lane, stile_g, 8 * u + 6);
    } else {
      unsigned ta[2];
      ta[0] = taddr_head(lane, 4, o0);
      ta[1] = taddr_head(lane, lcb1, o1);
      f32x16 acc[2][2] = {};
      if (type == 0) {
        k1_main<2, 2, 0, 6>(smem, w, wsrc, wvo, wstr, xsrc, xvo, ta, xaddr0, acc);
        k1_epi_q<2, 2>(acc, ws, lane, stile_g, 2 * cg);
      } else {
        k1_main<2, 2, 1, 6>(smem, w, wsrc, wvo, wstr, xsrc, xvo, ta, xaddr0, acc);
        asm volatile("s_waitcnt lgkmcnt(0)\n\ts_barrier" ::: "memory");
        k1_epi_kv<2, 2>(acc, smem, lane, sh, 2);
      }
    }
    if (type == 1) {
      if (qt != 3) asm volatile("s_waitcnt lgkmcnt(0)\n\ts_barrier" ::: "memory");
      asm volatile("s_waitcnt lgkmcnt(0)\n\ts_barrier" ::: "memory");
      if (tid < 256) {
        const f32x4 a = *(const f32x4*)(smem + tid * 16), c = *(const f32x4*)(smem + 4096 + tid * 16);
        *(f32x4*)(ws + WS_KV + (unsigned)(rg * 16 + hd) * 4096u + tid * 16) = a + c;
      }
    }
  } else {
    const char* wsrc[4];
    unsigned wstr[4], wvo[4];
    const int h0 = (cg == 14) ? 2 : 9;
    const int byte0 = 56 * h0, L0 = byte0 >> 7;
#pragma unroll
    for (int m = 0; m < 4; ++m) {
      const int lc = wh + 2 * m;
      wsrc[m] = (const char*)wqkv + ((lc < 4) ? 896 : 1792) + (L0 + (lc & 3)) * 128;
      wvo[m] = (unsigned)(drow * 2688 + ((dpos ^ dsw) << 4));
      wstr[m] = 32u * 2688u;
    }
    const int o0 = byte0 - L0 * 128;
    const int st = w & 3;
    const unsigned xaddr0 = (unsigned)((st * 32 + r) * 128 + ((hh ^ ((r >> 1) & 7)) << 4));
    if (wh == 0) {
      unsigned ta[6];
#pragma unroll
      for (int p = 0; p < 3; ++p) { ta[2 * p] = taddr_head(lane, 0, o0 + 56 * p); ta[2 * p + 1] = taddr_head(lane, 4, o0 + 56 * p); }
      f32x16 acc[1][6] = {};
      k1_main<1, 6, 1, 8>(smem, w, wsrc, wvo, wstr, xsrc, xvo, ta, xaddr0, acc);
      asm volatile("s_waitcnt lgkmcnt(0)\n\ts_barrier" ::: "memory");
      k1_epi_kv<1, 6>(acc, smem, lane, st, 4);
    } else {
      unsigned ta[4];
#pragma unroll
      for (int p = 0; p < 2; ++p) { ta[2 * p] = taddr_head(lane, 0, o0 + 56 * (3 + p)); ta[2 * p + 1] = taddr_head(lane, 4, o0 + 56 * (3 + p)); }
      f32x16 acc[1][4] = {};
      k1_main<1, 4, 1, 8>(smem, w, wsrc, wvo, wstr, xsrc, xvo, ta, xaddr0, acc);
      asm volatile("s_waitcnt lgkmcnt(0)\n\ts_barrier" ::: "memory");
      k1_epi_kv<1, 4>(acc, smem, lane, 12 + st, 4);
    }
    asm volatile("s_waitcnt lgkmcnt(0)\n\ts_barrier" ::: "memory");
    for (int c = tid; c < 5 * 256; c += 512) {
      const int hl = c >> 8, cc = c & 255;
      const char* p = smem + hl * 16384 + cc * 16;
      const f32x4 v = (*(const f32x4*)p + *(const f32x4*)(p + 4096)) + (*(const f32x4*)(p + 8192) + *(const f32x4*)(p + 12288));
      *(f32x4*)(ws + WS_KV + (unsigned)(rg * 16 + h0 + hl) * 4096u + cc * 16) = v;
    }
  }

  if (gwv < 1568) {
    u32x4 pv;
#pragma unroll
    for (int j = 0; j < 4; ++j) pv[j] = (unsigned)w2v[2 * j] | ((unsigned)w2v[2 * j + 1] << 16);
    *(u32x4*)(ws + WS_W + (unsigned)(rt_n * NKS + rt_t) * 1024u + lane * 16) = pv;
  }
  {
    float s = 0.f;
#pragma unroll
    for (int j = 0; j < 4; ++j) {
      s += __uint_as_float(gx[j] << 16) * __uint_as_float(gg[j] << 16);
      s += __uint_as_float(gx[j] & 0xffff0000u) * __uint_as_float(gg[j] & 0xffff0000u);
    }
    if (lane >= 56) s = 0.f;
#pragma unroll
    for (int o = 32; o > 0; o >>= 1) s += __shfl_xor(s, o);
    if (lane == 0) *(float*)(ws + WS_G + gwv * 4) = __builtin_amdgcn_rcpf(1.f + __expf(-s));
  }
}

constexpr int K2_LDS = 96 * 1024;
constexpr int K2_D = 6;

__global__ __launch_bounds__(256, 1) void gm_k2(const unsigned short* __restrict__ wo, char* __restrict__ ws,
                                                unsigned short* __restrict__ out) {
  extern __shared__ __attribute__((aligned(16))) char smem[];
  const int tid = threadIdx.x, lane = tid & 63;
  const int w = __builtin_amdgcn_readfirstlane(tid >> 6);
  const int b = blockIdx.x, xcd = b & 7, bi = b >> 3;
  const int hh = lane >> 5, r = lane & 31;
  const __amdgpu_buffer_rsrc_t rsrc = __builtin_amdgcn_make_buffer_rsrc(ws, 0, (int)WS_END, 0x00020000);
  gu32* cnt = (gu32*)(ws + WS_CNT);

  if (bi < 4) {
    const int p = xcd * 4 + bi, h = p >> 1, nh = p & 1;
    float* kvs = (float*)smem;
    unsigned short wov[2][28];
#pragma unroll
    for (int i = 0; i < 2; ++i) {
      const int ntl = (w + 4 * i < 7) ? w + 4 * i : 0;
      const unsigned short* src = wo + (size_t)(28 * h) * 448 + 32 * (7 * nh + ntl) + r;
#pragma unroll
      for (int e = 0; e < 28; ++e) wov[i][e] = src[e * 448];
    }
    f32x4 acc = {0.f, 0.f, 0.f, 0.f};
    {
      f32x4 v[16];
#pragma unroll
      for (int g = 0; g < 16; ++g) v[g] = *(const f32x4*)(ws + WS_KV + (unsigned)(g * 16 + h) * 4096u + tid * 16);
#pragma unroll
      for (int g = 0; g < 16; ++g) acc += v[g];
    }
    {
#pragma unroll
      for (int i = 0; i < 4; ++i) kvs[(i + 8 * w + 4 * hh) * 36 + r] = acc[i];
    }
    __syncthreads();
#pragma unroll
    for (int i = 0; i < 2; ++i) {
      if (w + 4 * i < 7) {
        const int nt = 7 * nh + w + 4 * i;
        float wf[28];
#pragma unroll
        for (int e = 0; e < 28; ++e) wf[e] = bf2f(wov[i][e]);
        f32x8 m[2];
#pragma unroll
        for (int reg = 0; reg < 16; ++reg) {
          const int d = (reg & 3) + 8 * (reg >> 2) + 4 * hh;
          const f32x4* row = (const f32x4*)(kvs + d * 36);
          float s = 0.f;
#pragma unroll
          for (int e4 = 0; e4 < 7; ++e4) {
            const f32x4 kq = row[e4];
            s += kq[0] * wf[4 * e4] + kq[1] * wf[4 * e4 + 1] + kq[2] * wf[4 * e4 + 2] + kq[3] * wf[4 * e4 + 3];
          }
          m[reg >> 3][reg & 7] = (reg >= 12 && hh) ? 0.f : s;
        }
        const unsigned off = WS_W + (unsigned)(nt * NKS + 112 + 2 * h) * 1024u + lane * 16;
        __builtin_amdgcn_raw_buffer_store_b128(__builtin_bit_cast(u32x4, pack8(m[0])), rsrc, off, 0, 16);
        __builtin_amdgcn_raw_buffer_store_b128(__builtin_bit_cast(u32x4, pack8(m[1])), rsrc, off + 1024, 0, 16);
      }
    }
    if (nh == 0 && w == 3 && lane < 4) {
      const int t = lane >> 1, zh = lane & 1;
      f32x8 z;
#pragma unroll
      for (int j = 0; j < 8; ++j) {
        const int d = 16 * t + 8 * (j >> 2) + 4 * zh + (j & 3);
        const float zv = kvs[d * 36 + 28];
        z[j] = (d < 28) ? zv : 0.f;
      }
      __builtin_amdgcn_raw_buffer_store_b128(__builtin_bit_cast(u32x4, pack8(z)), rsrc, WS_Z + (unsigned)(h * 64 + lane * 16), 0, 16);
    }
    asm volatile("s_waitcnt vmcnt(0)" ::: "memory");
    __syncthreads();
    if (tid == 0) __hip_atomic_fetch_add(cnt, 1u, __ATOMIC_RELAXED, __HIP_MEMORY_SCOPE_AGENT);
    return;
  }

  const int j7 = bi - 4;
  const int rt = xcd * 4 + j7 / 7, ct = j7 % 7;
  const char* A0 = ws + WS_H + (unsigned)(2 * rt * NKS) * 1024u + lane * 16;
  const char* B0 = ws + WS_W + (unsigned)(2 * ct * NKS) * 1024u + lane * 16;
  constexpr unsigned TS = NKS * 1024u;

  const float g0 = *(const float*)(ws + WS_G + (rt * 64 + r) * 4);
  const float g1 = *(const float*)(ws + WS_G + (rt * 64 + 32 + r) * 4);

  f32x16 acc[2][2] = {};
  bf16x8 a0[K2_D], a1[K2_D], b0[K2_D], b1[K2_D];
  auto ld = [&](int i) {
    const unsigned ko = (unsigned)(4 * i + w) * 1024u;
    a0[i % K2_D] = *(const bf16x8*)(A0 + ko);
    b0[i % K2_D] = *(const bf16x8*)(B0 + ko);
    a1[i % K2_D] = *(const bf16x8*)(A0 + TS + ko);
    b1[i % K2_D] = *(const bf16x8*)(B0 + TS + ko);
  };
#pragma unroll
  for (int i = 0; i < K2_D; ++i) ld(i);
  __builtin_amdgcn_sched_barrier(0);

  unsigned flagv = 0;
  bf16x8 pq[4][2][2];
  bf16x8 mf[4][2][2];
  bf16x8 zf[4][2];
  constexpr int I_POLL = 12, I_ATT = 18;
#pragma unroll
  for (int i = 0; i < 28; ++i) {
    if (i == I_POLL) flagv = __hip_atomic_load(cnt, __ATOMIC_RELAXED, __HIP_MEMORY_SCOPE_AGENT);
    if (i == I_ATT) {
      unsigned spins = 0;
      while (__builtin_amdgcn_readfirstlane(flagv) < 32u && spins < (1u << 24)) {
        __builtin_amdgcn_s_sleep(2);
        flagv = __hip_atomic_load(cnt, __ATOMIC_RELAXED, __HIP_MEMORY_SCOPE_AGENT);
        ++spins;
      }
      __builtin_amdgcn_fence(__ATOMIC_ACQUIRE, "wavefront");
      asm volatile("" ::: "memory");
#pragma unroll
      for (int hq = 0; hq < 4; ++hq)
#pragma unroll
        for (int t = 0; t < 2; ++t) {
          const unsigned ks = (unsigned)(112 + 8 * w + 2 * hq + t);
          const unsigned mo = WS_W + (unsigned)(2 * ct * NKS) * 1024u + ks * 1024u + lane * 16;
          mf[hq][t][0] = __builtin_bit_cast(bf16x8, __builtin_amdgcn_raw_buffer_load_b128(rsrc, mo, 0, 16));
          mf[hq][t][1] = __builtin_bit_cast(bf16x8, __builtin_amdgcn_raw_buffer_load_b128(rsrc, mo + TS, 0, 16));
          zf[hq][t] = __builtin_bit_cast(bf16x8, __builtin_amdgcn_raw_buffer_load_b128(rsrc, WS_Z + (unsigned)((4 * w + hq) * 64 + t * 32 + hh * 16), 0, 16));
          pq[hq][t][0] = *(const bf16x8*)(A0 + ks * 1024u);
          pq[hq][t][1] = *(const bf16x8*)(A0 + TS + ks * 1024u);
        }
      __builtin_amdgcn_sched_barrier(0);
    }
    const int sl = i % K2_D;
    acc[0][0] = MFMA32(b0[sl], a0[sl], acc[0][0]);
    acc[0][1] = MFMA32(b1[sl], a0[sl], acc[0][1]);
    acc[1][0] = MFMA32(b0[sl], a1[sl], acc[1][0]);
    acc[1][1] = MFMA32(b1[sl], a1[sl], acc[1][1]);
    if (i + K2_D < 28) ld(i + K2_D);
    __builtin_amdgcn_sched_barrier(0);
  }
  {
    const float f0 = 1.f - g0, f1 = 1.f - g1;
#pragma unroll
    for (int i = 0; i < 16; ++i) { acc[0][0][i] *= f0; acc[0][1][i] *= f0; acc[1][0][i] *= f1; acc[1][1][i] *= f1; }
  }
#pragma unroll
  for (int hq = 0; hq < 4; ++hq)
#pragma unroll
    for (int st = 0; st < 2; ++st) {
      f32x16 den = {}, p0 = {}, p1 = {};
#pragma unroll
      for (int t = 0; t < 2; ++t) {
        den = MFMA32(zf[hq][t], pq[hq][t][st], den);
        p0 = MFMA32(mf[hq][t][0], pq[hq][t][st], p0);
        p1 = MFMA32(mf[hq][t][1], pq[hq][t][st], p1);
      }
      const float sc = (st ? g1 : g0) / (den[0] + 1e-6f);
#pragma unroll
      for (int i = 0; i < 16; ++i) { acc[st][0][i] += sc * p0[i]; acc[st][1][i] += sc * p1[i]; }
      if (st == 1) __builtin_amdgcn_sched_barrier(0);
    }

#pragma unroll
  for (int tl = 0; tl < 4; ++tl) {
    if (tl != w) {
      const int slot = tl * 3 + (w > tl ? w - 1 : w);
      char* dst = smem + slot * 4096 + lane * 16;
#pragma unroll
      for (int g = 0; g < 4; ++g) {
        const f32x16& a = acc[tl >> 1][tl & 1];
        f32x4 v = {a[4 * g], a[4 * g + 1], a[4 * g + 2], a[4 * g + 3]};
        *(f32x4*)(dst + g * 1024) = v;
      }
    }
  }
  __syncthreads();
  f32x16 fin = (w == 0) ? acc[0][0] : ((w == 1) ? acc[0][1] : ((w == 2) ? acc[1][0] : acc[1][1]));
#pragma unroll
  for (int sI = 0; sI < 3; ++sI) {
    const char* src = smem + (w * 3 + sI) * 4096 + lane * 16;
#pragma unroll
    for (int g = 0; g < 4; ++g) {
      const f32x4 v = *(const f32x4*)(src + g * 1024);
      fin[4 * g] += v[0]; fin[4 * g + 1] += v[1]; fin[4 * g + 2] += v[2]; fin[4 * g + 3] += v[3];
    }
  }
  {
    unsigned short* orow = out + (size_t)(rt * 64 + (w >> 1) * 32 + r) * 448 + ct * 64 + (w & 1) * 32 + 4 * hh;
#pragma unroll
    for (int g = 0; g < 4; ++g) {
      f32x4 v = {fin[4 * g], fin[4 * g + 1], fin[4 * g + 2], fin[4 * g + 3]};
      *(u32x2*)(orow + 8 * g) = __builtin_bit_cast(u32x2, __builtin_convertvector(v, bf16x4b));
    }
  }
}

extern "C" void kernel_launch(void* const* d_in, const int* in_sizes, int n_in, void* d_out, int out_size, void* d_ws,
                              size_t ws_size, hipStream_t stream) {
  const unsigned short* x = (const unsigned short*)d_in[0];
  const unsigned short* wqkv = (const unsigned short*)d_in[1];
  const unsigned short* wo = (const unsigned short*)d_in[2];
  const unsigned short* w1 = (const unsigned short*)d_in[4];
  const unsigned short* w3 = (const unsigned short*)d_in[5];
  const unsigned short* w2 = (const unsigned short*)d_in[6];
  const unsigned short* gw = (const unsigned short*)d_in[7];
  (void)hipFuncSetAttribute((const void*)gm_k1, hipFuncAttributeMaxDynamicSharedMemorySize, K1_LDS);
  (void)hipFuncSetAttribute((const void*)gm_k2, hipFuncAttributeMaxDynamicSharedMemorySize, K2_LDS);
  gm_k1<<<256, 512, K1_LDS, stream>>>(x, wqkv, w1, w3, w2, gw, (char*)d_ws);
  gm_k2<<<256, 256, K2_LDS, stream>>>(wo, (char*)d_ws, (unsigned short*)d_out);
}
```

```cpp
#include <hip/hip_runtime.h>
#include <hip/hip_bf16.h>
#include <stdint.h>
#include <type_traits>

typedef __attribute__((ext_vector_type(8))) short bf16x8;
typedef __attribute__((ext_vector_type(4))) short s16x4;
typedef __attribute__((ext_vector_type(8))) __bf16 bf16x8b;
typedef __attribute__((ext_vector_type(4))) __bf16 bf16x4b;
typedef __attribute__((ext_vector_type(8))) float f32x8;
typedef __attribute__((ext_vector_type(4))) float f32x4;
typedef __attribute__((ext_vector_type(16))) float f32x16;
typedef __attribute__((ext_vector_type(4))) unsigned u32x4;
typedef __attribute__((ext_vector_type(2))) unsigned u32x2;
typedef __attribute__((address_space(3))) s16x4* lds_s16x4_ptr;
typedef __attribute__((address_space(3))) unsigned* lds_u32_ptr;
typedef __attribute__((address_space(1))) unsigned gu32;

#define DI __device__ __forceinline__
#define MFMA32(a, b, c) __builtin_amdgcn_mfma_f32_32x32x16_bf16((a), (b), (c), 0, 0, 0)

constexpr int NKS = 144;
constexpr unsigned WS_H = 0;
constexpr unsigned WS_W = WS_H + 64u * NKS * 1024u;
constexpr unsigned WS_KV = WS_W + 14u * NKS * 1024u;
constexpr unsigned WS_WO = WS_KV + 16u * 16u * 4096u;
constexpr unsigned WS_Z = WS_WO + 448u * 1024u;
constexpr unsigned WS_G = WS_Z + 1024u;
constexpr unsigned WS_CNT = WS_G + 8192u;
constexpr unsigned WS_END = WS_CNT + 8u * 256u;

#define STAMP(kid, idx) do {} while (0)
#define STAMP_DECL do {} while (0)
#define STAMP_FLUSH(kid) do {} while (0)
#define STAMP_ARG
#define STAMP_PASS
template <int B, int E, class F> DI void static_for(F&& f) {
  if constexpr (B < E) { f(std::integral_constant<int, B>{}); static_for<B + 1, E>(f); }
}
DI bf16x8 pack8(f32x8 v) { return __builtin_bit_cast(bf16x8, __builtin_convertvector(v, bf16x8b)); }
DI float bf2f(unsigned short u) { return __uint_as_float(((unsigned)u) << 16); }
DI float phi_f(float t) { return (t > 0.f ? t + 1.f : __expf(t)) + 1e-6f; }
DI void glds16(const char* sbase, unsigned voff, unsigned lds_dst) {
  unsigned keep;
  asm volatile("s_mov_b32 %0, m0\n\ts_mov_b32 m0, %3\n\ts_nop 0\n\tglobal_load_lds_dwordx4 %1, %2\n\ts_mov_b32 m0, %0"
               : "=&s"(keep) : "v"(voff), "s"(sbase), "s"(lds_dst) : "memory");
}

constexpr int K1_XOFF = 98304;
constexpr int K1_GW = K1_XOFF + 14 * 4096;
constexpr int K1_PF = K1_GW + 1024;
constexpr int K1_LDS = K1_PF + 1024;
#define K1_NPRE 4

template <int N> DI void wait_vm_bar() { asm volatile("s_waitcnt vmcnt(%0)\n\ts_barrier" ::"i"(N) : "memory"); }
DI void bar_only() { asm volatile("s_barrier" ::: "memory"); }
constexpr int k1_min3(int a, int b, int c) { return a < b ? (a < c ? a : c) : (b < c ? b : c); }
constexpr int k1_wI(int R, int t) { return t < 0 ? R - 2 : k1_min3(k1_wI(R, t - 1) + 2, t + R - 1, 27); }
constexpr int k1_xI(int R, int t) { return t < 0 ? R + 1 : k1_min3(k1_xI(R, t - 1) + 2, t + R + 2, 27); }
constexpr int k1_allowed(int WI, int R, int t) {
  const int needW = t + 1 > 27 ? 27 : t + 1, needX = 4 * (needW >> 2) + 3;
  int cnt = 0, mark = 0;
  for (int v = -3; v <= R - 2; ++v) {
    cnt += 1; if (v + 3 == needX) mark = cnt;
    if (v >= 0) { cnt += WI; if (v == needW) mark = cnt; }
  }
  for (int i = 0; i < t; ++i) {
    for (int n = k1_xI(R, i - 1) + 1; n <= k1_xI(R, i); ++n) { cnt += 1; if (n == needX) mark = cnt; }
    for (int k = k1_wI(R, i - 1) + 1; k <= k1_wI(R, i); ++k) { cnt += WI; if (k == needW) mark = cnt; }
  }
  return cnt - mark;
}

DI void glds4v(const char* vptr, unsigned lds_dst) {
  unsigned keep;
  asm volatile("s_mov_b32 %0, m0\n\ts_mov_b32 m0, %2\n\ts_nop 0\n\tglobal_load_lds_dword %1, off\n\ts_mov_b32 m0, %0"
               : "=&s"(keep) : "v"(vptr), "s"(lds_dst) : "memory");
}
DI float dot8(bf16x8 a, bf16x8 b, float c) {
  typedef __attribute__((ext_vector_type(2))) __bf16 bf16x2b;
  const u32x4 ua = __builtin_bit_cast(u32x4, a), ub = __builtin_bit_cast(u32x4, b);
#pragma unroll
  for (int j = 0; j < 4; ++j) {
    const unsigned pa = ua[j], pb = ub[j];
    c = __builtin_amdgcn_fdot2_f32_bf16(__builtin_bit_cast(bf16x2b, pa), __builtin_bit_cast(bf16x2b, pb), c, false);
  }
  return c;
}

struct K1Side {
  const char* pf;
  unsigned pf_step;
  const char* ch_src;
  unsigned ch_vo0, ch_vo1;
  const char* g_src;
  bool do_pf, do_pf2;
};

template <int NS, int NT, int KIND, int NLC, bool DMA>
DI void k1_main(char* smem, int d, const char* const (&wsrc)[NLC / 2], const unsigned (&wvo)[NLC / 2],
                const unsigned (&wstr)[NLC / 2], const char* xsrc, unsigned xvo, const char* gwsrc, unsigned gwvo,
                const K1Side& side, const unsigned (&taddr)[NT], unsigned xaddr0, int st0,
                f32x16 (&acc)[NS][NT] STAMP_ARG) {
  constexpr int WI = NLC / 2;
  constexpr int R = (NLC == 6) ? 8 : 6;
  constexpr int U = R + 6;
  constexpr int WSLOT = NLC * 2048;
  constexpr int CH_OFF = (NLC == 6) ? 6 * WSLOT : 5 * WSLOT;
  const unsigned lds0 = (unsigned)(uintptr_t)(__attribute__((address_space(3))) char*)smem;
  const unsigned wdst0 = lds0 + (unsigned)(d * 1024);
  const unsigned xdst0 = lds0 + (unsigned)(K1_XOFF + d * 1024);

  auto issueW = [&](int k, int slot) {
#pragma unroll
    for (int m = 0; m < WI; ++m)
      glds16(wsrc[m] + (size_t)((unsigned)k * wstr[m]), wvo[m], (unsigned)(slot * WSLOT) + wdst0 + m * 4096);
  };
  auto issueX = [&](int n, int unit) {
    glds16(xsrc + (size_t)((unsigned)(n & 3) * (32u * 896u) + (unsigned)(n >> 2) * 128u), xvo, xdst0 + (unsigned)(unit * 4096));
  };
  auto warm = [&](int c) { if (side.do_pf) glds4v(side.pf + (size_t)c * side.pf_step, lds0 + K1_PF + d * 256); };
  auto readX = [&](bf16x8 (&xf)[NS], int st, int unit0, int kk) {
    int un = unit0 + st; un = (un >= U) ? un - U : un;
    xf[st] = *(const bf16x8*)(smem + K1_XOFF + un * 4096 + (xaddr0 ^ (unsigned)(kk << 5)));
  };
  auto readW = [&](bf16x8 (&wf)[NT], int t, int slot) {
    const char* wb = smem + slot * WSLOT;
    s16x4 lo = __builtin_amdgcn_ds_read_tr16_b64_v4i16((lds_s16x4_ptr)(wb + taddr[t]));
    s16x4 hi = __builtin_amdgcn_ds_read_tr16_b64_v4i16((lds_s16x4_ptr)(wb + taddr[t] + 512));
    wf[t] = __builtin_shufflevector(lo, hi, 0, 1, 2, 3, 4, 5, 6, 7);
  };

  if constexpr (DMA) {
    bar_only();
    if (d == 0) glds16(gwsrc, gwvo, lds0 + K1_GW);
#pragma unroll
    for (int v = -3; v <= R - 2; ++v) {
      issueX(v + 3, v + 3);
      if (v >= 0) issueW(v, v);
    }
    STAMP(0, 4);
    wait_vm_bar<k1_allowed(WI, R, -1)>();
  } else {
#pragma unroll
    for (int c = 0; c < K1_NPRE; ++c) warm(c);
    asm volatile("s_waitcnt vmcnt(0)" ::: "memory");
    STAMP(0, 4);
    bar_only();
    bar_only();
  }
  STAMP(0, 1);
  bf16x8 xa[NS], wa[NT], xb[NS], wb2[NT];
#pragma unroll
  for (int st = 0; st < NS; ++st) readX(xa, st, st0, 0);
#pragma unroll
  for (int t = 0; t < NT; ++t) readW(wa, t, 0);

  auto step = [&](auto tc, bf16x8 (&xc)[NS], bf16x8 (&wc)[NT], bf16x8 (&xn)[NS], bf16x8 (&wn)[NT]) {
    constexpr int t = decltype(tc)::value;
    if constexpr (DMA) wait_vm_bar<k1_allowed(WI, R, t)>(); else bar_only();
    constexpr int rslot = (t + 1) % R;
    constexpr int runit = (4 * ((t + 1) >> 2)) % U;
    constexpr int kk = (t + 1) & 3;
    constexpr int NM = NS * NT;
    constexpr int NR = NS + NT;
    constexpr int RPS = (NR + NM - 3) / (NM - 2);
#pragma unroll
    for (int i = 0; i < NM; ++i) {
      const int tt = i / NS, st = i % NS;
      __builtin_amdgcn_sched_barrier(0);
      if constexpr (KIND == 0) acc[st][tt] = MFMA32(wc[tt], xc[st], acc[st][tt]);
      else acc[st][tt] = MFMA32(xc[st], wc[tt], acc[st][tt]);
      __builtin_amdgcn_sched_barrier(0);
      if constexpr (t < 27) {
#pragma unroll
        for (int q = i * RPS; q < (i + 1) * RPS && q < NR; ++q) {
          if (q == 0) readX(xn, 0, runit + st0, kk);
          else if (q == 1) readW(wn, 0, rslot);
          else if (NS == 2 && q == 2) readX(xn, NS - 1, runit + st0, kk);
          else readW(wn, q - NS, rslot);
        }
      }
      if constexpr (DMA) {
        constexpr int xlo = k1_xI(R, t - 1) + 1, xhi = k1_xI(R, t), wlo = k1_wI(R, t - 1) + 1, whi = k1_wI(R, t);
        if (i == NM - 3) {
#pragma unroll
          for (int n = xlo; n <= xhi; ++n) issueX(n, n % U);
        }
        if (i == NM - 2) {
#pragma unroll
          for (int k = wlo; k <= whi; ++k) issueW(k, k % R);
        }
      } else {
        if (i == NM - 2) {
          if constexpr (t % 3 == 1 && K1_NPRE + t / 3 < 7) warm(K1_NPRE + t / 3);
          if constexpr (t == 14) { if (side.do_pf2) glds4v(side.pf, lds0 + K1_PF + d * 256); }
          if constexpr (t == 25) {
            glds16(side.ch_src, side.ch_vo0, lds0 + CH_OFF + d * 2048);
            glds16(side.ch_src, side.ch_vo1, lds0 + CH_OFF + d * 2048 + 1024);
            glds16(side.g_src, gwvo, lds0 + CH_OFF + 8192 + d * 2048);
            glds16(side.g_src + 896, gwvo, lds0 + CH_OFF + 8192 + d * 2048 + 1024);
          }
        }
      }
    }
    __builtin_amdgcn_sched_barrier(0);
  };
  static_for<0, 14>([&](auto pc) {
    constexpr int t = 2 * decltype(pc)::value;
    step(std::integral_constant<int, t>{}, xa, wa, xb, wb2);
    step(std::integral_constant<int, t + 1>{}, xb, wb2, xa, wa);
  });
  STAMP(0, 2);
}

DI unsigned taddr_ffn(int lane, int i) {
  const int hh = lane >> 5, blk = (lane >> 4) & 1, q = (lane & 15) >> 2, p = lane & 3;
  const int lc = (i >> 2) + 2 * blk, inoff = (i & 3) * 32 + 8 * p;
  const int pos = (inoff >> 4) ^ (4 * ((q >> 1) & 1)) ^ (2 * blk);
  return (unsigned)(lc * 2048 + (8 * hh + q) * 128 + pos * 16 + (inoff & 15));
}
DI unsigned taddr_head(int lane, int lcb, int o) {
  const int hh = lane >> 5, blk = (lane >> 4) & 1, q = (lane & 15) >> 2, p = lane & 3;
  const int pp = (blk == 1 && p == 3) ? 2 : p;
  const int vo = o + 32 * blk + 8 * pp;
  const int lc = lcb + (vo >> 7), inoff = vo & 127;
  const int pos = (inoff >> 4) ^ (4 * ((q >> 1) & 1));
  return (unsigned)(lc * 2048 + (8 * hh + q) * 128 + pos * 16 + (inoff & 15));
}

template <int NS, int NT>
DI void k1_epi_ffn(f32x16 (&acc)[NS][NT], char* ws, int lane, int stile_g, int kstep0) {
#pragma unroll
  for (int st = 0; st < NS; ++st)
#pragma unroll
    for (int t = 0; t < NT; ++t) {
      f32x8 hv;
#pragma unroll
      for (int j = 0; j < 8; ++j) {
        const float gt = acc[st][t][j], up = acc[st][t][j + 8];
        hv[j] = gt * __builtin_amdgcn_rcpf(1.f + __expf(-gt)) * up;
      }
      *(bf16x8*)(ws + WS_H + (unsigned)((stile_g + st) * NKS + kstep0 + t) * 1024u + lane * 16) = pack8(hv);
    }
}

template <int NS, int NT>
DI void k1_epi_q(f32x16 (&acc)[NS][NT], char* ws, int lane, int stile_g, int head0) {
  const bool hi = lane >= 32;
#pragma unroll
  for (int st = 0; st < NS; ++st)
#pragma unroll
    for (int t = 0; t < NT; ++t) {
      f32x8 a, b;
#pragma unroll
      for (int j = 0; j < 8; ++j) {
        a[j] = phi_f(acc[st][t][j]);
        const float pb = phi_f(acc[st][t][j + 8]);
        b[j] = (j >= 4 && hi) ? 0.f : pb;
      }
      char* dst = ws + WS_H + (unsigned)((stile_g + st) * NKS + 112 + 2 * (head0 + t)) * 1024u + lane * 16;
      *(bf16x8*)dst = pack8(a);
      *(bf16x8*)(dst + 1024) = pack8(b);
    }
}

template <int NS, int NT>
DI void k1_epi_kv(f32x16 (&acc)[NS][NT], char* smem, int lane, int slot0, int slot_stride) {
  const int r = lane & 31;
#pragma unroll
  for (int pi = 0; pi < NT / 2; ++pi) {
    f32x16 kv = {};
#pragma unroll
    for (int st = 0; st < NS; ++st)
#pragma unroll
      for (int t = 0; t < 2; ++t) {
        f32x8 pk, vv;
#pragma unroll
        for (int j = 0; j < 8; ++j) {
          const float kk = phi_f(acc[st][2 * pi][8 * t + j]);
          pk[j] = (r < 28) ? kk : 0.f;
          const float v0 = acc[st][2 * pi + 1][8 * t + j];
          vv[j] = (r < 28) ? v0 : ((r == 28) ? 1.f : 0.f);
        }
        kv = MFMA32(pack8(pk), pack8(vv), kv);
      }
    char* dst = smem + (slot0 + pi * slot_stride) * 4096 + lane * 16;
#pragma unroll
    for (int g = 0; g < 4; ++g) {
      f32x4 v = {kv[4 * g], kv[4 * g + 1], kv[4 * g + 2], kv[4 * g + 3]};
      *(f32x4*)(dst + g * 1024) = v;
    }
  }
}

__global__ __launch_bounds__(512, 2) void gm_k1(const unsigned short* __restrict__ x, const unsigned short* __restrict__ wqkv,
                                                const unsigned short* __restrict__ w1, const unsigned short* __restrict__ w3,
                                                const unsigned short* __restrict__ w2, const unsigned short* __restrict__ wo,
                                                const unsigned short* __restrict__ gw, char* __restrict__ ws) {
  extern __shared__ __attribute__((aligned(16))) char smem[];
  const int tid = threadIdx.x, lane = tid & 63;
  const int w = __builtin_amdgcn_readfirstlane(tid >> 6);
  const int b = blockIdx.x, xcd = b & 7, bi = b >> 3;
  const int rg = 8 * (xcd & 1) + (bi & 7);
  const int cq = 4 * (xcd >> 1) + (bi >> 3);
  const int cg = (cq < 8) ? (((cq & 3) == 3) ? 14 + (cq >> 2) : 3 * (cq >> 2) + (cq & 3)) : cq - 2;
  const int type = (cg < 8) ? 0 : ((cg < 14) ? 1 : 2);

  STAMP_DECL;
  STAMP(0, 0);
  const int hh = lane >> 5, r = lane & 31;
  const int wh = w >> 2, d = w & 3;

  int hd = 0, o0 = 0, o1 = 0, lcb1 = 4, offA = 0, offB = 0;
  if (type == 0) {
    const int byte0 = 112 * cg, L0 = byte0 >> 7, L1 = (byte0 + 111) >> 7;
    offA = L0 * 128; offB = L1 * 128;
    o0 = byte0 - L0 * 128; o1 = o0 + 56; lcb1 = 4;
  } else {
    const int i6 = cg - 8;
    hd = (type == 2) ? ((cg == 14) ? 2 : 9) : ((i6 < 2) ? i6 : ((i6 < 4) ? i6 + 5 : i6 + 10));
    const int byte0 = 56 * hd, L0 = byte0 >> 7;
    offA = 896 + L0 * 128; offB = 1792 + L0 * 128;
    o0 = byte0 - L0 * 128; o1 = o0; lcb1 = 5;
  }
  const char* xsrc = (const char*)x + (size_t)(rg * 128) * 896;
  const int drow = 8 * (d & 1) + (lane >> 3);
  const int dpos = lane & 7, dsw = 4 * ((lane >> 4) & 1);
  const unsigned xvo = (unsigned)((8 * d + (lane >> 3)) * 896 + ((dpos ^ ((4 * d + (lane >> 4)) & 7)) << 4));
  const unsigned gwvo = (unsigned)((lane < 56 ? lane : 55) * 16);
  if (b == 0 && tid < 8) *(unsigned*)(ws + WS_CNT + tid * 256) = 0u;

  const int slab = b * 4 + d;
  const int sl2 = (slab < 1008) ? slab : 0;
  const bool is_wo = sl2 >= 784;
  const int sl_a = is_wo ? (sl2 - 784) / 7 : sl2 / 7;
  const int sl_np = (is_wo ? sl2 - 784 : sl2) - sl_a * 7;
  const int sl_row0 = is_wo ? 28 * (sl_a >> 1) + 16 * (sl_a & 1) : 16 * sl_a;
  const int sl_rmax = is_wo ? 447 : 1791;
  K1Side side;
  side.ch_src = (const char*)(is_wo ? wo : w2) + sl_np * 128;
  {
    const int r0 = sl_row0 + (lane >> 3), r1 = r0 + 8;
    side.ch_vo0 = (unsigned)((r0 < sl_rmax ? r0 : sl_rmax) * 896 + ((dpos ^ dsw) << 4));
    side.ch_vo1 = (unsigned)((r1 < sl_rmax ? r1 : sl_rmax) * 896 + ((dpos ^ dsw) << 4));
  }
  side.g_src = (const char*)x + (size_t)(slab * 2) * 896;
  side.do_pf = (w < 2); side.do_pf2 = (w == 2);
  side.pf = nullptr; side.pf_step = 0;
  if (w == 0) {
    side.pf = (const char*)x + (size_t)((xcd & 1) * 1024 + bi * 32 + r) * 896;
    side.pf_step = 128u;
  } else if (w == 1) {
    const int lc = lane >> 3, kr = (bi & 7) * 8 + (lane & 7);
    if (type != 2) {
      const int lcc = lc < 6 ? lc : 5;
      const char* base = (lcc < 2) ? (const char*)w1 + 256 * cg + 128 * lcc
                       : (lcc < 4) ? (const char*)w3 + 256 * cg + 128 * (lcc - 2) : (const char*)wqkv + (lcc == 4 ? offA : offB);
      const unsigned str = (lcc < 4) ? 3584u : 2688u;
      side.pf = base + (size_t)kr * str; side.pf_step = 64u * str;
    } else {
      side.pf = (const char*)wqkv + ((lc < 4) ? offA : offB) + (lc & 3) * 128 + (size_t)kr * 2688; side.pf_step = 64u * 2688u;
    }
  } else if (w == 2) {
    const int s4 = b * 4 + (lane >> 4), s42 = (s4 < 1008) ? s4 : 0;
    const bool wo4 = s42 >= 784;
    const int a4 = wo4 ? (s42 - 784) / 7 : s42 / 7, np4 = (wo4 ? s42 - 784 : s42) - a4 * 7;
    int row = (wo4 ? 28 * (a4 >> 1) + 16 * (a4 & 1) : 16 * a4) + (lane & 15);
    row = (wo4 && row > 447) ? 447 : row;
    side.pf = (const char*)(wo4 ? wo : w2) + (size_t)row * 896 + np4 * 128;
  }
  auto chores_finish = [&](int ch_off) {
    asm volatile("s_waitcnt vmcnt(0)" ::: "memory");
    const int blk = (lane >> 4) & 1, q = (lane & 15) >> 2, p = lane & 3;
    const int rlo = (is_wo ? 8 * hh : 4 * hh) + q, rstep = is_wo ? 4 : 8;
#pragma unroll
    for (int nt2 = 0; nt2 < 2; ++nt2) {
      const int inoff = nt2 * 64 + blk * 32 + 8 * p;
      const int pos = (inoff >> 4) ^ (4 * ((q >> 1) & 1));
      const char* src = smem + ch_off + d * 2048 + rlo * 128 + pos * 16 + (inoff & 15);
      s16x4 lo = __builtin_amdgcn_ds_read_tr16_b64_v4i16((lds_s16x4_ptr)src);
      s16x4 hi = __builtin_amdgcn_ds_read_tr16_b64_v4i16((lds_s16x4_ptr)(src + rstep * 128));
      if (is_wo && (sl_a & 1) && hh) hi = s16x4{0, 0, 0, 0};
      const bf16x8 fr = __builtin_shufflevector(lo, hi, 0, 1, 2, 3, 4, 5, 6, 7);
      const int nt = 2 * sl_np + nt2;
      const unsigned dst = is_wo ? WS_WO + (unsigned)((sl_a >> 1) * 28 + nt * 2 + (sl_a & 1)) * 1024u
                                 : WS_W + (unsigned)(nt * NKS + sl_a) * 1024u;
      if (slab < 1008) *(bf16x8*)(ws + dst + lane * 16) = fr;
    }
    const bf16x8 gv = *(const bf16x8*)(smem + K1_GW + lane * 16);
#pragma unroll
    for (int i = 0; i < 2; ++i) {
      const bf16x8 xv = *(const bf16x8*)(smem + ch_off + 8192 + d * 2048 + i * 1024 + lane * 16);
      float sg = dot8(xv, gv, 0.f);
      if (lane >= 56) sg = 0.f;
#pragma unroll
      for (int o = 32; o > 0; o >>= 1) sg += __shfl_xor(sg, o);
      if (lane == 0) *(float*)(ws + WS_G + (slab * 2 + i) * 4) = fminf(fmaxf(sg, -60.f), 60.f);
    }
  };

  const unsigned xaddr0 = (unsigned)(r * 128 + ((hh ^ ((r >> 1) & 7)) << 4));
  if (type != 2) {
    const char* wsrc[3];
    unsigned wstr[3], wvo[3];
    wsrc[0] = (const char*)w1 + 256 * cg + 128 * (d >> 1);
    wsrc[1] = (const char*)w3 + 256 * cg + 128 * (d >> 1);
    wsrc[2] = (const char*)wqkv + ((d >> 1) ? offB : offA);
    wvo[0] = (unsigned)(drow * 3584 + ((dpos ^ dsw) << 4));
    wvo[1] = (unsigned)(drow * 3584 + ((dpos ^ dsw ^ 2) << 4));
    wvo[2] = (unsigned)(drow * 2688 + ((dpos ^ dsw) << 4));
    wstr[0] = wstr[1] = 16u * 3584u; wstr[2] = 16u * 2688u;
    const int sh = w & 1, qt = w >> 1;
    const int stile_g = rg * 4 + sh * 2;
    if (qt < 2) {
      unsigned ta[3];
#pragma unroll
      for (int t = 0; t < 3; ++t) ta[t] = taddr_ffn(lane, 3 * qt + t);
      f32x16 acc[2][3] = {};
      k1_main<2, 3, 0, 6, false>(smem, d, wsrc, wvo, wstr, xsrc, xvo, (const char*)gw, gwvo, side, ta, xaddr0, 2 * sh, acc STAMP_PASS);
      k1_epi_ffn<2, 3>(acc, ws, lane, stile_g, 8 * cg + 3 * qt);
      chores_finish(6 * 12288);
    } else if (qt == 2) {
      unsigned ta[2];
#pragma unroll
      for (int t = 0; t < 2; ++t) ta[t] = taddr_ffn(lane, 6 + t);
      f32x16 acc[2][2] = {};
      k1_main<2, 2, 0, 6, true>(smem, d, wsrc, wvo, wstr, xsrc, xvo, (const char*)gw, gwvo, side, ta, xaddr0, 2 * sh, acc STAMP_PASS);
      k1_epi_ffn<2, 2>(acc, ws, lane, stile_g, 8 * cg + 6);
    } else {
      unsigned ta[2];
      ta[0] = taddr_head(lane, 4, o0);
      ta[1] = taddr_head(lane, lcb1, o1);
      f32x16 acc[2][2] = {};
      if (type == 0) {
        k1_main<2, 2, 0, 6, true>(smem, d, wsrc, wvo, wstr, xsrc, xvo, (const char*)gw, gwvo, side, ta, xaddr0, 2 * sh, acc STAMP_PASS);
        k1_epi_q<2, 2>(acc, ws, lane, stile_g, 2 * cg);
      } else {
        k1_main<2, 2, 1, 6, true>(smem, d, wsrc, wvo, wstr, xsrc, xvo, (const char*)gw, gwvo, side, ta, xaddr0, 2 * sh, acc STAMP_PASS);
        k1_epi_kv<2, 2>(acc, smem, lane, sh, 2);
      }
    }
    if (type == 1) {
      asm volatile("s_waitcnt lgkmcnt(0)\n\ts_barrier" ::: "memory");
      if (tid >= 256) {
        const int c = tid - 256;
        const f32x4 a = *(const f32x4*)(smem + c * 16), e = *(const f32x4*)(smem + 4096 + c * 16);
        *(f32x4*)(ws + WS_KV + (unsigned)(rg * 16 + hd) * 4096u + c * 16) = a + e;
      }
    }
  } else {
    const char* wsrc[4];
    unsigned wstr[4], wvo[4];
#pragma unroll
    for (int m = 0; m < 4; ++m) {
      wsrc[m] = (const char*)wqkv + ((m < 2) ? offA : offB) + (((d >> 1) + 2 * m) & 3) * 128;
      wvo[m] = (unsigned)(drow * 2688 + ((dpos ^ dsw) << 4));
      wstr[m] = 16u * 2688u;
    }
    if (wh == 0) {
      unsigned ta[6];
#pragma unroll
      for (int p = 0; p < 3; ++p) { ta[2 * p] = taddr_head(lane, 0, o0 + 56 * p); ta[2 * p + 1] = taddr_head(lane, 4, o0 + 56 * p); }
      f32x16 acc[1][6] = {};
      k1_main<1, 6, 1, 8, false>(smem, d, wsrc, wvo, wstr, xsrc, xvo, (const char*)gw, gwvo, side, ta, xaddr0, d, acc STAMP_PASS);
      asm volatile("s_waitcnt lgkmcnt(0)\n\ts_barrier" ::: "memory");
      k1_epi_kv<1, 6>(acc, smem, lane, d, 4);
      chores_finish(5 * 16384);
    } else {
      unsigned ta[4];
#pragma unroll
      for (int p = 0; p < 2; ++p) { ta[2 * p] = taddr_head(lane, 0, o0 + 56 * (3 + p)); ta[2 * p + 1] = taddr_head(lane, 4, o0 + 56 * (3 + p)); }
      f32x16 acc[1][4] = {};
      k1_main<1, 4, 1, 8, true>(smem, d, wsrc, wvo, wstr, xsrc, xvo, (const char*)gw, gwvo, side, ta, xaddr0, d, acc STAMP_PASS);
      asm volatile("s_waitcnt lgkmcnt(0)\n\ts_barrier" ::: "memory");
      k1_epi_kv<1, 4>(acc, smem, lane, 12 + d, 4);
    }
    asm volatile("s_waitcnt lgkmcnt(0)\n\ts_barrier" ::: "memory");
    for (int c = tid; c < 5 * 256; c += 512) {
      const int hl = c >> 8, cc = c & 255;
      const char* p = smem + hl * 16384 + cc * 16;
      const f32x4 v = (*(const f32x4*)p + *(const f32x4*)(p + 4096)) + (*(const f32x4*)(p + 8192) + *(const f32x4*)(p + 12288));
      *(f32x4*)(ws + WS_KV + (unsigned)(rg * 16 + hd + hl) * 4096u + cc * 16) = v;
    }
  }
  STAMP(0, 3);
  STAMP_FLUSH(0);
}

constexpr int K2_LDS = 96 * 1024;
constexpr int K2_D = 5;

__global__ __launch_bounds__(256, 2) void gm_k2(char* __restrict__ ws, unsigned short* __restrict__ out) {
  extern __shared__ __attribute__((aligned(16))) char smem[];
  const int tid = threadIdx.x, lane = tid & 63;
  const int w = __builtin_amdgcn_readfirstlane(tid >> 6);
  const int b = blockIdx.x, xcd = b & 7, bi = b >> 3;
  const int hh = lane >> 5, r = lane & 31;
  const __amdgpu_buffer_rsrc_t rsrc = __builtin_amdgcn_make_buffer_rsrc(ws, 0, (int)WS_END, 0x00020000);

  STAMP_DECL;
  STAMP(1, 0);
  if (bi < 4) {
    const int p = xcd * 4 + bi, h = p >> 1, nh = p & 1;
    float* kvs = (float*)smem;
    f32x4 v[16];
#pragma unroll
    for (int g = 0; g < 16; ++g) v[g] = *(const f32x4*)(ws + WS_KV + (unsigned)(g * 16 + h) * 4096u + tid * 16);
    bf16x8 wov[2][2];
#pragma unroll
    for (int i = 0; i < 2; ++i) {
      const int ntl = (w + 4 * i < 7) ? w + 4 * i : 0;
#pragma unroll
      for (int t = 0; t < 2; ++t)
        wov[i][t] = *(const bf16x8*)(ws + WS_WO + (unsigned)(h * 28 + (7 * nh + ntl) * 2 + t) * 1024u + lane * 16);
    }
    f32x4 acc = {0.f, 0.f, 0.f, 0.f};
#pragma unroll
    for (int g = 0; g < 16; ++g) acc += v[g];
#pragma unroll
    for (int i = 0; i < 4; ++i) kvs[(i + 8 * w + 4 * hh) * 36 + r] = acc[i];
    __syncthreads();
    STAMP(1, 1);
    bf16x8 ahi[2], alo[2];
#pragma unroll
    for (int t = 0; t < 2; ++t) {
      const f32x4 k0 = *(const f32x4*)(kvs + r * 36 + 16 * t + 8 * hh), k1 = *(const f32x4*)(kvs + r * 36 + 16 * t + 8 * hh + 4);
      f32x8 kf, lo;
#pragma unroll
      for (int j = 0; j < 8; ++j) {
        const float kvv = (j < 4) ? k0[j & 3] : k1[j & 3];
        kf[j] = (r < 28 && !(t == 1 && hh == 1 && j >= 4)) ? kvv : 0.f;
      }
      ahi[t] = pack8(kf);
#pragma unroll
      for (int j = 0; j < 8; ++j) lo[j] = kf[j] - bf2f((unsigned short)ahi[t][j]);
      alo[t] = pack8(lo);
    }
#pragma unroll
    for (int i = 0; i < 2; ++i) {
      if (w + 4 * i < 7) {
        const int nt = 7 * nh + w + 4 * i;
        f32x16 m = {};
#pragma unroll
        for (int t = 0; t < 2; ++t) {
          m = MFMA32(ahi[t], wov[i][t], m);
          m = MFMA32(alo[t], wov[i][t], m);
        }
        f32x8 m0, m1;
#pragma unroll
        for (int j = 0; j < 8; ++j) { m0[j] = m[j]; m1[j] = m[j + 8]; }
        const unsigned off = WS_W + (unsigned)(nt * NKS + 112 + 2 * h) * 1024u + lane * 16;
        __builtin_amdgcn_raw_buffer_store_b128(__builtin_bit_cast(u32x4, pack8(m0)), rsrc, off, 0, 16);
        __builtin_amdgcn_raw_buffer_store_b128(__builtin_bit_cast(u32x4, pack8(m1)), rsrc, off + 1024, 0, 16);
      }
    }
    if (nh == 0 && w == 3 && lane < 4) {
      const int t = lane >> 1, zh = lane & 1;
      f32x8 z;
#pragma unroll
      for (int j = 0; j < 8; ++j) {
        const int d = 16 * t + 8 * (j >> 2) + 4 * zh + (j & 3);
        const float zv = kvs[d * 36 + 28];
        z[j] = (d < 28) ? zv : 0.f;
      }
      __builtin_amdgcn_raw_buffer_store_b128(__builtin_bit_cast(u32x4, pack8(z)), rsrc, WS_Z + (unsigned)(h * 64 + lane * 16), 0, 16);
    }
    asm volatile("s_waitcnt vmcnt(0)" ::: "memory");
    __syncthreads();
    if (tid < 8) __hip_atomic_fetch_add((gu32*)(ws + WS_CNT + tid * 256), 1u, __ATOMIC_RELAXED, __HIP_MEMORY_SCOPE_AGENT);
    STAMP(1, 5);
    STAMP_FLUSH(1);
    return;
  }

  gu32* cnt = (gu32*)(ws + WS_CNT + xcd * 256);
  const int j7 = bi - 4;
  const int rt = xcd * 4 + j7 / 7, ct = j7 % 7;
  const char* A0 = ws + WS_H + (unsigned)(2 * rt * NKS) * 1024u + lane * 16;
  const char* B0 = ws + WS_W + (unsigned)(2 * ct * NKS) * 1024u + lane * 16;
  const unsigned Boff = WS_W + (unsigned)(2 * ct * NKS) * 1024u + lane * 16;
  constexpr unsigned TS = NKS * 1024u;

  const float g0 = __expf(*(const float*)(ws + WS_G + (rt * 64 + r) * 4));
  const float g1 = __expf(*(const float*)(ws + WS_G + (rt * 64 + 32 + r) * 4));

  f32x16 acc[2][2] = {};
  bf16x8 a0[K2_D], a1[K2_D], b0[K2_D], b1[K2_D], zf[K2_D];
  constexpr int I_A = 22;
  auto ld = [&](int i) {
    if (i < I_A || i >= I_A + 8) {
      const unsigned ko = (unsigned)(4 * (i < I_A ? i : i - 8) + w) * 1024u;
      a0[i % K2_D] = *(const bf16x8*)(A0 + ko);
      b0[i % K2_D] = *(const bf16x8*)(B0 + ko);
      a1[i % K2_D] = *(const bf16x8*)(A0 + TS + ko);
      b1[i % K2_D] = *(const bf16x8*)(B0 + TS + ko);
    } else {
      const unsigned ko = (unsigned)(112 + 8 * w + (i - I_A)) * 1024u;
      a0[i % K2_D] = *(const bf16x8*)(A0 + ko);
      b0[i % K2_D] = __builtin_bit_cast(bf16x8, __builtin_amdgcn_raw_buffer_load_b128(rsrc, Boff + ko, 0, 16));
      a1[i % K2_D] = *(const bf16x8*)(A0 + TS + ko);
      b1[i % K2_D] = __builtin_bit_cast(bf16x8, __builtin_amdgcn_raw_buffer_load_b128(rsrc, Boff + TS + ko, 0, 16));
      zf[i % K2_D] = __builtin_bit_cast(bf16x8, __builtin_amdgcn_raw_buffer_load_b128(rsrc, WS_Z + (unsigned)((8 * w + (i - I_A)) * 32 + hh * 16), 0, 16));
    }
  };
#pragma unroll
  for (int i = 0; i < K2_D; ++i) ld(i);
  __builtin_amdgcn_sched_barrier(0);

  unsigned flagv = 0;
  constexpr int I_CHK = I_A - K2_D, I_POLL = I_CHK - K2_D + 1;
#pragma unroll
  for (int i = 0; i < 36; ++i) {
    if (i == 1) STAMP(1, 6);
    if (i == I_POLL) flagv = __hip_atomic_load(cnt, __ATOMIC_RELAXED, __HIP_MEMORY_SCOPE_AGENT);
    const int sl = i % K2_D;
    if (i < I_A || i >= I_A + 8) {
      acc[0][0] = MFMA32(b0[sl], a0[sl], acc[0][0]);
      acc[0][1] = MFMA32(b1[sl], a0[sl], acc[0][1]);
      acc[1][0] = MFMA32(b0[sl], a1[sl], acc[1][0]);
      acc[1][1] = MFMA32(b1[sl], a1[sl], acc[1][1]);
      if (i == I_CHK) {
        STAMP(1, 1);
        unsigned spins = 0;
        while (__builtin_amdgcn_readfirstlane(flagv) < 32u && spins < (1u << 22)) {
          __builtin_amdgcn_s_sleep(8);
          flagv = __hip_atomic_load(cnt, __ATOMIC_RELAXED, __HIP_MEMORY_SCOPE_AGENT);
          ++spins;
        }
        __builtin_amdgcn_fence(__ATOMIC_ACQUIRE, "wavefront");
        asm volatile("" ::: "memory");
        STAMP(1, 2);
      }
      if (i + K2_D < 36) ld(i + K2_D);
    } else if (((i - I_A) & 1) == 1) {
      const int s0 = (i - 1) % K2_D, s1 = sl;
#pragma unroll
      for (int st = 0; st < 2; ++st) {
        f32x16 den = {}, p0 = {}, p1 = {};
        den = MFMA32(zf[s0], st ? a1[s0] : a0[s0], den);
        p0 = MFMA32(b0[s0], st ? a1[s0] : a0[s0], p0);
        p1 = MFMA32(b1[s0], st ? a1[s0] : a0[s0], p1);
        den = MFMA32(zf[s1], st ? a1[s1] : a0[s1], den);
        p0 = MFMA32(b0[s1], st ? a1[s1] : a0[s1], p0);
        p1 = MFMA32(b1[s1], st ? a1[s1] : a0[s1], p1);
        const float sc = (st ? g1 : g0) * __builtin_amdgcn_rcpf(den[0] + 1e-6f);
#pragma unroll
        for (int e = 0; e < 16; ++e) { acc[st][0][e] += sc * p0[e]; acc[st][1][e] += sc * p1[e]; }
        asm volatile("" : "+v"(acc[st][0]), "+v"(acc[st][1]));
        __builtin_amdgcn_sched_barrier(0);
      }
      ld(i - 1 + K2_D);
      ld(i + K2_D);
      if (i == I_A + 7) STAMP(1, 3);
    }
    __builtin_amdgcn_sched_barrier(0);
  }

  STAMP(1, 4);
#pragma unroll
  for (int tl = 0; tl < 4; ++tl) {
    if (tl != w) {
      const int slot = tl * 3 + (w > tl ? w - 1 : w);
      char* dst = smem + slot * 4096 + lane * 16;
#pragma unroll
      for (int g = 0; g < 4; ++g) {
        const f32x16& a = acc[tl >> 1][tl & 1];
        f32x4 v = {a[4 * g], a[4 * g + 1], a[4 * g + 2], a[4 * g + 3]};
        *(f32x4*)(dst + g * 1024) = v;
      }
    }
  }
  STAMP(1, 7);
  __syncthreads();
  f32x16 fin = (w == 0) ? acc[0][0] : ((w == 1) ? acc[0][1] : ((w == 2) ? acc[1][0] : acc[1][1]));
#pragma unroll
  for (int sI = 0; sI < 3; ++sI) {
    const char* src = smem + (w * 3 + sI) * 4096 + lane * 16;
#pragma unroll
    for (int g = 0; g < 4; ++g) {
      const f32x4 v = *(const f32x4*)(src + g * 1024);
      fin[4 * g] += v[0]; fin[4 * g + 1] += v[1]; fin[4 * g + 2] += v[2]; fin[4 * g + 3] += v[3];
    }
  }
  {
    const float omg = __builtin_amdgcn_rcpf(1.f + ((w >> 1) ? g1 : g0));
    unsigned short* orow = out + (size_t)(rt * 64 + (w >> 1) * 32 + r) * 448 + ct * 64 + (w & 1) * 32 + 4 * hh;
#pragma unroll
    for (int g = 0; g < 4; ++g) {
      f32x4 v = {fin[4 * g] * omg, fin[4 * g + 1] * omg, fin[4 * g + 2] * omg, fin[4 * g + 3] * omg};
      *(u32x2*)(orow + 8 * g) = __builtin_bit_cast(u32x2, __builtin_convertvector(v, bf16x4b));
    }
  }
  STAMP(1, 5);
  STAMP_FLUSH(1);
}

extern "C" void kernel_launch(void* const* d_in, const int* in_sizes, int n_in, void* d_out, int out_size, void* d_ws,
                              size_t ws_size, hipStream_t stream) {
  const unsigned short* x = (const unsigned short*)d_in[0];
  const unsigned short* wqkv = (const unsigned short*)d_in[1];
  const unsigned short* wo = (const unsigned short*)d_in[2];
  const unsigned short* w1 = (const unsigned short*)d_in[4];
  const unsigned short* w3 = (const unsigned short*)d_in[5];
  const unsigned short* w2 = (const unsigned short*)d_in[6];
  const unsigned short* gw = (const unsigned short*)d_in[7];
  (void)hipFuncSetAttribute((const void*)gm_k1, hipFuncAttributeMaxDynamicSharedMemorySize, K1_LDS);
  (void)hipFuncSetAttribute((const void*)gm_k2, hipFuncAttributeMaxDynamicSharedMemorySize, K2_LDS);
  gm_k1<<<256, 512, K1_LDS, stream>>>(x, wqkv, w1, w3, w2, wo, gw, (char*)d_ws);
  gm_k2<<<256, 256, K2_LDS, stream>>>((char*)d_ws, (unsigned short*)d_out);
}
```

```cpp
#include <hip/hip_runtime.h>
#include <hip/hip_bf16.h>
#include <stdint.h>
#include <type_traits>

typedef __attribute__((ext_vector_type(8))) short bf16x8;
typedef __attribute__((ext_vector_type(4))) short s16x4;
typedef __attribute__((ext_vector_type(8))) __bf16 bf16x8b;
typedef __attribute__((ext_vector_type(4))) __bf16 bf16x4b;
typedef __attribute__((ext_vector_type(8))) float f32x8;
typedef __attribute__((ext_vector_type(4))) float f32x4;
typedef __attribute__((ext_vector_type(16))) float f32x16;
typedef __attribute__((ext_vector_type(4))) unsigned u32x4;
typedef __attribute__((ext_vector_type(2))) unsigned u32x2;
typedef __attribute__((address_space(3))) s16x4* lds_s16x4_ptr;
typedef __attribute__((address_space(3))) unsigned* lds_u32_ptr;
typedef __attribute__((address_space(1))) unsigned gu32;
typedef __attribute__((address_space(1))) unsigned long long gu64;
typedef unsigned long long u64;

#define DI __device__ __forceinline__
#define MFMA32(a, b, c) __builtin_amdgcn_mfma_f32_32x32x16_bf16((a), (b), (c), 0, 0, 0)

constexpr int NKS = 144;
constexpr unsigned WS_H = 0;
constexpr unsigned WS_W = WS_H + 64u * NKS * 1024u;
constexpr unsigned WS_KV = WS_W + 14u * NKS * 1024u;
constexpr unsigned WS_WO = WS_KV + 32u * 16u * 4096u;
constexpr unsigned WS_Z = WS_WO + 448u * 1024u;
constexpr unsigned WS_G = WS_Z + 1024u;
constexpr unsigned WS_EP = WS_G + 8192u;
constexpr unsigned WS_F0 = WS_EP + 256u;
constexpr unsigned WS_F1 = WS_F0 + 2048u;
constexpr unsigned WS_FM = WS_F1 + 16384u;
constexpr unsigned WS_END = WS_FM + 256u;

#define STAMP(kid, idx) do {} while (0)
#define STAMP_DECL do {} while (0)
#define STAMP_FLUSH(kid) do {} while (0)
#define STAMP_ARG
#define STAMP_PASS
template <int B, int E, class F> DI void static_for(F&& f) {
  if constexpr (B < E) { f(std::integral_constant<int, B>{}); static_for<B + 1, E>(f); }
}
DI bf16x8 pack8(f32x8 v) { return __builtin_bit_cast(bf16x8, __builtin_convertvector(v, bf16x8b)); }
DI float bf2f(unsigned short u) { return __uint_as_float(((unsigned)u) << 16); }
DI float phi_f(float t) { return (t > 0.f ? t + 1.f : __expf(t)) + 1e-6f; }
DI void glds16(const char* sbase, unsigned voff, unsigned lds_dst) {
  unsigned keep;
  asm volatile("s_mov_b32 %0, m0\n\ts_mov_b32 m0, %3\n\ts_nop 0\n\tglobal_load_lds_dwordx4 %1, %2\n\ts_mov_b32 m0, %0"
               : "=&s"(keep) : "v"(voff), "s"(sbase), "s"(lds_dst) : "memory");
}

#define RSRC __amdgpu_buffer_rsrc_t
DI void st16(RSRC rs, unsigned off, bf16x8 v) { __builtin_amdgcn_raw_buffer_store_b128(__builtin_bit_cast(u32x4, v), rs, off, 0, 16); }
DI void st16f(RSRC rs, unsigned off, f32x4 v) { __builtin_amdgcn_raw_buffer_store_b128(__builtin_bit_cast(u32x4, v), rs, off, 0, 16); }
DI bf16x8 ld16(RSRC rs, unsigned off) { return __builtin_bit_cast(bf16x8, __builtin_amdgcn_raw_buffer_load_b128(rs, off, 0, 16)); }
#define EXP_AUXA 16
#define EXP_AUXB 16
DI bf16x8 ld16a(RSRC rs, unsigned off) { return __builtin_bit_cast(bf16x8, __builtin_amdgcn_raw_buffer_load_b128(rs, off, 0, EXP_AUXA)); }
DI bf16x8 ld16b(RSRC rs, unsigned off) { return __builtin_bit_cast(bf16x8, __builtin_amdgcn_raw_buffer_load_b128(rs, off, 0, EXP_AUXB)); }
DI f32x4 ld16f(RSRC rs, unsigned off) { return __builtin_bit_cast(f32x4, __builtin_amdgcn_raw_buffer_load_b128(rs, off, 0, 16)); }
DI u64 ldflag(const char* p) { return __hip_atomic_load((gu64*)p, __ATOMIC_RELAXED, __HIP_MEMORY_SCOPE_AGENT); }
DI void stflag(char* p, u64 v) { __hip_atomic_store((gu64*)p, v, __ATOMIC_RELAXED, __HIP_MEMORY_SCOPE_AGENT); }
template <int NL> DI void wait_flags(const char* p, int lane, int cnt, int stride, u64 tag) {
#pragma nounroll
  for (unsigned spins = 0; spins < (1u << 20); ++spins) {
    bool ok = true;
#pragma unroll
    for (int i = 0; i < NL; ++i) { const int ix = i * 64 + lane; ok &= (ldflag(p + (ix < cnt ? ix : 0) * stride) == tag); }
    if (__all(ok)) break;
    __builtin_amdgcn_s_sleep(4);
  }
  __builtin_amdgcn_fence(__ATOMIC_ACQUIRE, "wavefront");
  asm volatile("" ::: "memory");
}

constexpr int K1_R = 8, K1_U = K1_R + 6, K1_WSLOT = 5 * 2048;
constexpr int K1_XOFF = K1_R * K1_WSLOT;
constexpr int K1_CH = K1_XOFF + K1_U * 4096;
constexpr int K1_GW = K1_CH + 16384;
constexpr int K1_PF = K1_GW + 4096;
constexpr int K1_LDS = K1_PF + 1024;
#define K1_NPRE 4

template <int N> DI void wait_vm_bar() { asm volatile("s_waitcnt vmcnt(%0)\n\ts_barrier" ::"i"(N) : "memory"); }
DI void bar_only() { asm volatile("s_barrier" ::: "memory"); }
constexpr int k1_allowed(int WI, int XI, int t) {
  const int needW = t + 1 > 27 ? 27 : t + 1, needX = 4 * (needW >> 2) + 3;
  const int tt = t < 0 ? 0 : t;
  const int lastW = tt - 1 + K1_R - 1 > 27 ? 27 : tt - 1 + K1_R - 1, lastX = tt - 1 + K1_R + 2 > 27 ? 27 : tt - 1 + K1_R + 2;
  return WI * (lastW - needW) + XI * (lastX - needX);
}

DI void glds4v(const char* vptr, unsigned lds_dst) {
  unsigned keep;
  asm volatile("s_mov_b32 %0, m0\n\ts_mov_b32 m0, %2\n\ts_nop 0\n\tglobal_load_lds_dword %1, off\n\ts_mov_b32 m0, %0"
               : "=&s"(keep) : "v"(vptr), "s"(lds_dst) : "memory");
}
DI float wave_sum63(float v) {
#define GM_DPP(ctrl, rm) v += __builtin_bit_cast(float, __builtin_amdgcn_update_dpp(0, __builtin_bit_cast(int, v), ctrl, rm, 0xf, false))
  GM_DPP(0x111, 0xf); GM_DPP(0x112, 0xf); GM_DPP(0x114, 0xf); GM_DPP(0x118, 0xf);
  GM_DPP(0x142, 0xa); GM_DPP(0x143, 0xc);
#undef GM_DPP
  return v;
}
DI float dot8(bf16x8 a, bf16x8 b, float c) {
  typedef __attribute__((ext_vector_type(2))) __bf16 bf16x2b;
  const u32x4 ua = __builtin_bit_cast(u32x4, a), ub = __builtin_bit_cast(u32x4, b);
#pragma unroll
  for (int j = 0; j < 4; ++j) {
    const unsigned pa = ua[j], pb = ub[j];
    c = __builtin_amdgcn_fdot2_f32_bf16(__builtin_bit_cast(bf16x2b, pa), __builtin_bit_cast(bf16x2b, pb), c, false);
  }
  return c;
}

template <int NT, int KIND, int WI, int XI, class SIDE>
DI void k1_main(char* smem, int d, const char* const (&wsrc)[3], const unsigned (&wvo)[3], const unsigned (&wstr)[3],
                const char* xsrc, unsigned xvo, unsigned xvo2, SIDE&& side, const unsigned (&taddr)[NT], unsigned xaddr0, int st0,
                f32x16 (&acc)[2][NT] STAMP_ARG) {
  constexpr int NS = 2, R = K1_R, U = K1_U, WSLOT = K1_WSLOT;
  constexpr bool DMA = WI + XI > 0;
  const unsigned lds0 = (unsigned)(uintptr_t)(__attribute__((address_space(3))) char*)smem;
  const unsigned wdst0 = lds0 + (unsigned)(d * 1024);
  const unsigned xdst0 = lds0 + (unsigned)(K1_XOFF + (d & 1) * 2048);

  auto issueW = [&](int k, int slot) {
#pragma unroll
    for (int m = 0; m < WI; ++m)
      glds16(wsrc[m] + (size_t)((unsigned)k * wstr[m]), wvo[m], (unsigned)(slot * WSLOT) + wdst0 + m * 4096);
  };
  auto issueX = [&](int n, int unit) {
    if constexpr (XI > 0) {
      const char* src = xsrc + (size_t)((unsigned)(n & 3) * (32u * 896u) + (unsigned)(n >> 2) * 128u);
      glds16(src, xvo, xdst0 + (unsigned)(unit * 4096));
      glds16(src, xvo2, xdst0 + (unsigned)(unit * 4096) + 1024);
    }
  };
  auto readX = [&](bf16x8 (&xf)[NS], int st, int unit0, int kk) {
    int un = unit0 + st; un = (un >= U) ? un - U : un;
    xf[st] = *(const bf16x8*)(smem + K1_XOFF + un * 4096 + (xaddr0 ^ (unsigned)(kk << 5)));
  };
  auto readW = [&](bf16x8 (&wf)[NT], int t, int slot) {
    const char* wb = smem + slot * WSLOT;
    s16x4 lo = __builtin_amdgcn_ds_read_tr16_b64_v4i16((lds_s16x4_ptr)(wb + taddr[t]));
    s16x4 hi = __builtin_amdgcn_ds_read_tr16_b64_v4i16((lds_s16x4_ptr)(wb + taddr[t] + 512));
    wf[t] = __builtin_shufflevector(lo, hi, 0, 1, 2, 3, 4, 5, 6, 7);
  };

  if constexpr (DMA) {
    bar_only();
#pragma unroll
    for (int e = -3; e <= R - 2; ++e) {
      issueX(e + 3, e + 3);
      if (e >= 0) issueW(e, e);
    }
    STAMP(0, 4);
    wait_vm_bar<k1_allowed(WI, XI, -1)>();
  } else {
    asm volatile("s_waitcnt vmcnt(0)" ::: "memory");
    STAMP(0, 4);
    bar_only();
    bar_only();
  }
  STAMP(0, 1);
  bf16x8 xa[NS], wa[NT], xb[NS], wb2[NT];
#pragma unroll
  for (int st = 0; st < NS; ++st) readX(xa, st, st0, 0);
#pragma unroll
  for (int t = 0; t < NT; ++t) readW(wa, t, 0);

  auto step = [&](auto tc, bf16x8 (&xc)[NS], bf16x8 (&wc)[NT], bf16x8 (&xn)[NS], bf16x8 (&wn)[NT]) {
    constexpr int t = decltype(tc)::value;
    if constexpr (DMA) wait_vm_bar<k1_allowed(WI, XI, t)>(); else bar_only();
    constexpr int rslot = (t + 1) % R;
    constexpr int runit = (4 * ((t + 1) >> 2)) % U;
    constexpr int kk = (t + 1) & 3;
    constexpr int NM = NS * NT;
    constexpr int NR = NS + NT;
    constexpr int RPS = (NR + NM - 3) / (NM - 2);
#pragma unroll
    for (int i = 0; i < NM; ++i) {
      const int tt = i / NS, st = i % NS;
      __builtin_amdgcn_sched_barrier(0);
      if constexpr (KIND == 0) acc[st][tt] = MFMA32(wc[tt], xc[st], acc[st][tt]);
      else acc[st][tt] = MFMA32(xc[st], wc[tt], acc[st][tt]);
      __builtin_amdgcn_sched_barrier(0);
      if constexpr (t < 27) {
#pragma unroll
        for (int q = i * RPS; q < (i + 1) * RPS && q < NR; ++q) {
          if (q == 0) readX(xn, 0, runit + st0, kk);
          else if (q == 1) readW(wn, 0, rslot);
          else if (q == 2) readX(xn, 1, runit + st0, kk);
          else readW(wn, q - NS, rslot);
        }
      }
      if constexpr (DMA) {
        if (i == NM - 2) { if constexpr (t + R + 2 < 28) issueX(t + R + 2, (t + R + 2) % U); }
        if (i == NM - 1) { if constexpr (t + R - 1 < 28) issueW(t + R - 1, (t + R - 1) % R); }
      } else {
        if (i == NM - 2) side(tc);
      }
    }
    __builtin_amdgcn_sched_barrier(0);
  };
  static_for<0, 14>([&](auto pc) {
    constexpr int t = 2 * decltype(pc)::value;
    step(std::integral_constant<int, t>{}, xa, wa, xb, wb2);
    step(std::integral_constant<int, t + 1>{}, xb, wb2, xa, wa);
  });
  STAMP(0, 2);
}

DI unsigned taddr_ffn(int lane, int i) {
  const int hh = lane >> 5, blk = (lane >> 4) & 1, q = (lane & 15) >> 2, p = lane & 3;
  const int lc = (i >> 2) + 2 * blk, inoff = (i & 3) * 32 + 8 * p;
  const int pos = (inoff >> 4) ^ (4 * ((q >> 1) & 1)) ^ (2 * blk);
  return (unsigned)(lc * 2048 + (8 * hh + q) * 128 + pos * 16 + (inoff & 15));
}
DI unsigned taddr_head(int lane, int lcb, int o) {
  const int hh = lane >> 5, blk = (lane >> 4) & 1, q = (lane & 15) >> 2, p = lane & 3;
  const int pp = (blk == 1 && p == 3) ? 2 : p;
  const int vo = o + 32 * blk + 8 * pp;
  const int lc = lcb + (vo >> 7), inoff = vo & 127;
  const int pos = (inoff >> 4) ^ (4 * ((q >> 1) & 1));
  return (unsigned)(lc * 2048 + (8 * hh + q) * 128 + pos * 16 + (inoff & 15));
}

template <int NS, int NT>
DI void k1_epi_ffn(f32x16 (&acc)[NS][NT], RSRC rs, int lane, int stile_g, int kstep0) {
#pragma unroll
  for (int st = 0; st < NS; ++st)
#pragma unroll
    for (int t = 0; t < NT; ++t) {
      f32x8 hv;
#pragma unroll
      for (int j = 0; j < 8; ++j) {
        const float gt = acc[st][t][j], up = acc[st][t][j + 8];
        hv[j] = gt * __builtin_amdgcn_rcpf(1.f + __expf(-gt)) * up;
      }
      st16(rs, WS_H + (unsigned)((stile_g + st) * NKS + kstep0 + t) * 1024u + lane * 16, pack8(hv));
    }
}

template <int NS, int NT>
DI void k1_epi_q(f32x16 (&acc)[NS][NT], RSRC rs, int lane, int stile_g, int head0) {
  const bool hi = lane >= 32;
#pragma unroll
  for (int st = 0; st < NS; ++st)
#pragma unroll
    for (int t = 0; t < NT; ++t) {
      f32x8 a, b;
#pragma unroll
      for (int j = 0; j < 8; ++j) {
        a[j] = phi_f(acc[st][t][j]);
        const float pb = phi_f(acc[st][t][j + 8]);
        b[j] = (j >= 4 && hi) ? 0.f : pb;
      }
      const unsigned dst = WS_H + (unsigned)((stile_g + st) * NKS + 112 + 2 * (head0 + t)) * 1024u + lane * 16;
      st16(rs, dst, pack8(a));
      st16(rs, dst + 1024, pack8(b));
    }
}

DI void k1_epi_kv(f32x16 (&acc)[2][2], RSRC rs, int lane, unsigned dst) {
  const int r = lane & 31;
  f32x16 kv = {};
#pragma unroll
  for (int st = 0; st < 2; ++st)
#pragma unroll
    for (int t = 0; t < 2; ++t) {
      f32x8 pk, vv;
#pragma unroll
      for (int j = 0; j < 8; ++j) {
        const float kk = phi_f(acc[st][0][8 * t + j]);
        pk[j] = (r < 28) ? kk : 0.f;
        const float v0 = acc[st][1][8 * t + j];
        vv[j] = (r < 28) ? v0 : ((r == 28) ? 1.f : 0.f);
      }
      kv = MFMA32(pack8(pk), pack8(vv), kv);
    }
#pragma unroll
  for (int g = 0; g < 4; ++g) {
    f32x4 v = {kv[4 * g], kv[4 * g + 1], kv[4 * g + 2], kv[4 * g + 3]};
    st16f(rs, dst + g * 1024 + lane * 16, v);
  }
}

constexpr int K2_D = 5;
#define K2_IA 28

__global__ __launch_bounds__(512, 2) void gm_fused(const unsigned short* __restrict__ x, const unsigned short* __restrict__ wqkv,
                                                   const unsigned short* __restrict__ w1, const unsigned short* __restrict__ w3,
                                                   const unsigned short* __restrict__ w2, const unsigned short* __restrict__ wo,
                                                   const unsigned short* __restrict__ gw, char* __restrict__ ws,
                                                   unsigned short* __restrict__ out) {
  extern __shared__ __attribute__((aligned(16))) char smem[];
  const int tid = threadIdx.x, lane = tid & 63;
  const int w = __builtin_amdgcn_readfirstlane(tid >> 6);
  const int b = blockIdx.x, xcd = b & 7, bi = b >> 3;
  const int rg = 8 * (xcd & 1) + (bi & 7);
  const int cq = 4 * (xcd >> 1) + (bi >> 3);
  const int cg = (cq < 8) ? (((cq & 3) == 3) ? 14 + (cq >> 2) : 3 * (cq >> 2) + (cq & 3)) : cq - 2;
  const bool isq = cg >= 14;
  const RSRC rs = __builtin_amdgcn_make_buffer_rsrc(ws, 0, (int)WS_END, 0x00020000);
  const unsigned lds0 = (unsigned)(uintptr_t)(__attribute__((address_space(3))) char*)smem;

  STAMP_DECL;
  STAMP(0, 0);
  const int hh = lane >> 5, r = lane & 31;
  const int d = w & 3;
  const int kvw = (56 * cg) & ~15;
  const int qoff = (cg - 14) * 384;
  const char* xsrc = (const char*)x + (size_t)(rg * 128) * 896;
  const int drow = 8 * (d & 1) + (lane >> 3);
  const int dpos = lane & 7, dsw = 4 * ((lane >> 4) & 1);
  const unsigned xvo = (unsigned)((16 * (d & 1) + (lane >> 3)) * 896 + ((dpos ^ ((lane >> 4) & 7)) << 4));
  const unsigned xvo2 = (unsigned)((16 * (d & 1) + 8 + (lane >> 3)) * 896 + ((dpos ^ ((4 + (lane >> 4)) & 7)) << 4));

  const char* pf = nullptr;
  unsigned pf_step = 0;
  if (w == 0) {
    pf = (const char*)x + (size_t)((xcd & 1) * 1024 + bi * 32 + r) * 896;
    pf_step = 128u;
  } else if (w == 1) {
    const int lc = lane >> 3, kr = (bi & 7) * 8 + (lane & 7);
    const bool ffn = !isq && lc < 4;
    const char* base = ffn ? (const char*)(lc < 2 ? w1 : w3) + 256 * cg + 128 * (lc & 1)
                     : (const char*)wqkv + ((lc < 4) ? qoff + 128 * lc : ((lc < 6) ? 896 : 1792) + kvw + (lc & 1) * 60);
    const unsigned str = ffn ? 3584u : 2688u;
    pf = base + (size_t)kr * str; pf_step = 64u * str;
  }
  if (w < 2) {
#pragma unroll
    for (int c = 0; c < K1_NPRE; ++c) glds4v(pf + (size_t)c * pf_step, lds0 + K1_PF + d * 256);
  }
  if (w == 0) {
    u64 pc; unsigned len;
    asm volatile("s_getpc_b64 %0\n.Lgm_pc_here:\n\ts_mov_b32 %1, .Lgm_code_end-.Lgm_pc_here" : "=s"(pc), "=s"(len));
    const unsigned off = (unsigned)(bi * 16 + (lane & 15)) * 128u;
    glds4v((const char*)pc + ((int)len > 0 && off < len ? off : 0u), lds0 + K1_PF + d * 256);
  }
  const u64 tag = ldflag(ws + WS_EP) + 1ull;

  auto side = [&](auto tc) {
    constexpr int t = decltype(tc)::value;
    const unsigned stg = (unsigned)(K1_CH + d * 8192);
    if constexpr (t % 3 == 1 && K1_NPRE + t / 3 < 7) glds4v(pf + (size_t)(K1_NPRE + t / 3) * pf_step, lds0 + K1_PF + d * 256);
    if constexpr (t == 2 || t == 3 || t == 15 || t == 16) {
      constexpr int i = (t == 2 || t == 15) ? 0 : 1;
      const int slab = b * 4 + d * 2 + i, sl2 = (slab < 1008) ? slab : 0;
      const bool is_wo = sl2 >= 784;
      const int sl_a = is_wo ? (sl2 - 784) / 7 : sl2 / 7;
      const int sl_np = (is_wo ? sl2 - 784 : sl2) - sl_a * 7;
      if constexpr (t < 4) {
        const int row0 = is_wo ? 28 * (sl_a >> 1) + 16 * (sl_a & 1) : 16 * sl_a;
        const int rmax = is_wo ? 447 : 1791;
        const char* src = (const char*)(is_wo ? wo : w2) + sl_np * 128;
        const int r0 = row0 + (lane >> 3), r1 = r0 + 8;
        glds16(src, (unsigned)((r0 < rmax ? r0 : rmax) * 896 + ((dpos ^ dsw) << 4)), lds0 + stg + i * 2048);
        glds16(src, (unsigned)((r1 < rmax ? r1 : rmax) * 896 + ((dpos ^ dsw) << 4)), lds0 + stg + i * 2048 + 1024);
      } else {
        if constexpr (t == 15) asm volatile("s_waitcnt vmcnt(0)" ::: "memory");
        const int blk = (lane >> 4) & 1, q = (lane & 15) >> 2, p = lane & 3;
        const int rlo = (is_wo ? 8 * hh : 4 * hh) + q, rstep = is_wo ? 4 : 8;
#pragma unroll
        for (int nt2 = 0; nt2 < 2; ++nt2) {
          const int inoff = nt2 * 64 + blk * 32 + 8 * p;
          const int pos = (inoff >> 4) ^ (4 * ((q >> 1) & 1));
          const char* src = smem + stg + i * 2048 + rlo * 128 + pos * 16 + (inoff & 15);
          s16x4 lo = __builtin_amdgcn_ds_read_tr16_b64_v4i16((lds_s16x4_ptr)src);
          s16x4 hi = __builtin_amdgcn_ds_read_tr16_b64_v4i16((lds_s16x4_ptr)(src + rstep * 128));
          if (is_wo && (sl_a & 1) && hh) hi = s16x4{0, 0, 0, 0};
          const bf16x8 fr = __builtin_shufflevector(lo, hi, 0, 1, 2, 3, 4, 5, 6, 7);
          const int nt = 2 * sl_np + nt2;
          const unsigned dst = is_wo ? WS_WO + (unsigned)((sl_a >> 1) * 28 + nt * 2 + (sl_a & 1)) * 1024u
                                     : WS_W + (unsigned)(nt * NKS + sl_a) * 1024u;
          if (slab < 1008) st16(rs, dst + lane * 16, fr);
        }
      }
    }
    if constexpr (t == 5 || t == 6 || t == 8) {
      const unsigned gwvo = (unsigned)((lane < 56 ? lane : 55) * 16);
      const char* xr = (const char*)x + (size_t)(b * 8 + d * 4) * 896;
      if constexpr (t == 8) glds16((const char*)gw, gwvo, lds0 + K1_GW + d * 1024);
      else {
        glds16(xr + (t - 5) * 1792, gwvo, lds0 + stg + 4096 + (t - 5) * 2048);
        glds16(xr + (t - 5) * 1792 + 896, gwvo, lds0 + stg + 4096 + (t - 5) * 2048 + 1024);
      }
    }
    if constexpr (t == 17 || t == 18) {
      const bf16x8 gv = *(const bf16x8*)(smem + K1_GW + d * 1024 + lane * 16);
      const bf16x8 xv0 = *(const bf16x8*)(smem + stg + 4096 + (t - 17) * 2048 + lane * 16);
      const bf16x8 xv1 = *(const bf16x8*)(smem + stg + 4096 + (t - 17) * 2048 + 1024 + lane * 16);
      float s0 = dot8(xv0, gv, 0.f), s1 = dot8(xv1, gv, 0.f);
      if (lane >= 56) { s0 = 0.f; s1 = 0.f; }
      s0 = wave_sum63(s0); s1 = wave_sum63(s1);
      if (lane == 63) {
        const u32x2 lg = {__builtin_bit_cast(unsigned, fminf(fmaxf(s0, -60.f), 60.f)), __builtin_bit_cast(unsigned, fminf(fmaxf(s1, -60.f), 60.f))};
        __builtin_amdgcn_raw_buffer_store_b64(lg, rs, WS_G + (unsigned)(b * 8 + d * 4 + (t - 17) * 2) * 4u, 0, 16);
      }
    }
    if constexpr (t == 23) asm volatile("s_waitcnt vmcnt(0)" ::: "memory");
    if constexpr (t == 24) { if (tid == 0) stflag(ws + WS_F0 + b * 8, tag); }
  };
  auto noside = [&](auto) {};

  const unsigned xaddr0 = (unsigned)(r * 128 + ((hh ^ ((r >> 1) & 7)) << 4));
  {
    const char* wsrc[3];
    unsigned wstr[3], wvo[3];
    const unsigned str = isq ? 2688u : 3584u;
    wsrc[0] = isq ? (const char*)wqkv + qoff + 128 * (d >> 1) : (const char*)w1 + 256 * cg + 128 * (d >> 1);
    wsrc[1] = isq ? (const char*)wqkv + qoff + 256 + 128 * (d >> 1) : (const char*)w3 + 256 * cg + 128 * (d >> 1);
    wsrc[2] = (const char*)wqkv + 896 + kvw;
    wvo[0] = (unsigned)drow * str + (unsigned)((dpos ^ dsw) << 4);
    wvo[1] = (unsigned)drow * str + (unsigned)((dpos ^ dsw ^ (isq ? 0 : 2)) << 4);
    { const int c = dpos ^ dsw; wvo[2] = (unsigned)(drow * 2688 + ((c < 4) ? 0 : 896) + (c & 3) * 16); }
    wstr[0] = wstr[1] = 16u * str; wstr[2] = 16u * 2688u;
    const int sh = w & 1, qt = w >> 1;
    const int stile_g = rg * 4 + sh * 2;
    const int qo0 = (cg - 14) * 64;
    if (qt < 2) {
      unsigned ta[3];
#pragma unroll
      for (int t = 0; t < 3; ++t) ta[t] = isq ? taddr_head(lane, 0, qo0 + 56 * (3 * qt + t)) : taddr_ffn(lane, 3 * qt + t);
      f32x16 acc[2][3] = {};
      if (qt == 0) k1_main<3, 0, 0, 0>(smem, d, wsrc, wvo, wstr, xsrc, xvo, xvo2, side, ta, xaddr0, 2 * sh, acc STAMP_PASS);
      else k1_main<3, 0, 0, 2>(smem, d, wsrc, wvo, wstr, xsrc, xvo, xvo2, noside, ta, xaddr0, 2 * sh, acc STAMP_PASS);
      if (isq) k1_epi_q<2, 3>(acc, rs, lane, stile_g, 8 * (cg - 14) + 3 * qt);
      else k1_epi_ffn<2, 3>(acc, rs, lane, stile_g, 8 * cg + 3 * qt);
    } else if (qt == 2) {
      unsigned ta[2];
#pragma unroll
      for (int t = 0; t < 2; ++t) ta[t] = isq ? taddr_head(lane, 0, qo0 + 56 * (6 + t)) : taddr_ffn(lane, 6 + t);
      f32x16 acc[2][2] = {};
      k1_main<2, 0, 3, 0>(smem, d, wsrc, wvo, wstr, xsrc, xvo, xvo2, noside, ta, xaddr0, 2 * sh, acc STAMP_PASS);
      if (isq) k1_epi_q<2, 2>(acc, rs, lane, stile_g, 8 * (cg - 14) + 6);
      else k1_epi_ffn<2, 2>(acc, rs, lane, stile_g, 8 * cg + 6);
    } else {
      unsigned ta[2];
      ta[0] = taddr_head(lane, 4, (56 * cg) & 15);
      ta[1] = taddr_head(lane, 4, 64 + ((56 * cg) & 15));
      f32x16 acc[2][2] = {};
      k1_main<2, 1, 2, 0>(smem, d, wsrc, wvo, wstr, xsrc, xvo, xvo2, noside, ta, xaddr0, 2 * sh, acc STAMP_PASS);
      k1_epi_kv(acc, rs, lane, WS_KV + (unsigned)((rg * 2 + sh) * 16 + cg) * 4096u);
    }
  }
  STAMP(0, 3);
  asm volatile("s_waitcnt vmcnt(0)" ::: "memory");
  if (lane == 0) stflag(ws + WS_F1 + (((rg * 2 + (w & 1)) * 16 + cg) * 4 + (w >> 1)) * 8, tag);
  STAMP_FLUSH(0);
  if (w < 4) return;

  const int w2i = w - 4, tid2 = tid - 256;
  STAMP(1, 0);
  wait_flags<4>(ws + WS_F0, lane, 256, 8, tag);
  if (b == 32 && tid2 == 0) stflag(ws + WS_EP, tag);

  if (bi < 4) {
    const int p = xcd * 4 + bi, h = p >> 1, nh = p & 1;
    float* kvs = (float*)smem;
    bf16x8 wov[2][2];
#pragma unroll
    for (int i = 0; i < 2; ++i) {
      const int ntl = (w2i + 4 * i < 7) ? w2i + 4 * i : 0;
#pragma unroll
      for (int t = 0; t < 2; ++t) wov[i][t] = ld16(rs, WS_WO + (unsigned)(h * 28 + (7 * nh + ntl) * 2 + t) * 1024u + lane * 16);
    }
    wait_flags<1>(ws + WS_F1 + (h * 4 + 3) * 8, lane, 32, 512, tag);
    f32x4 v[32];
#pragma unroll
    for (int g = 0; g < 32; ++g) v[g] = ld16f(rs, WS_KV + (unsigned)(g * 16 + h) * 4096u + tid2 * 16);
    f32x4 acc = {0.f, 0.f, 0.f, 0.f};
#pragma unroll
    for (int g = 0; g < 32; ++g) acc += v[g];
#pragma unroll
    for (int i = 0; i < 4; ++i) kvs[(i + 8 * w2i + 4 * hh) * 36 + r] = acc[i];
    __syncthreads();
    STAMP(1, 1);
    bf16x8 ahi[2], alo[2];
#pragma unroll
    for (int t = 0; t < 2; ++t) {
      const f32x4 k0 = *(const f32x4*)(kvs + r * 36 + 16 * t + 8 * hh), k1 = *(const f32x4*)(kvs + r * 36 + 16 * t + 8 * hh + 4);
      f32x8 kf, lo;
#pragma unroll
      for (int j = 0; j < 8; ++j) {
        const float kvv = (j < 4) ? k0[j & 3] : k1[j & 3];
        kf[j] = (r < 28 && !(t == 1 && hh == 1 && j >= 4)) ? kvv : 0.f;
      }
      ahi[t] = pack8(kf);
#pragma unroll
      for (int j = 0; j < 8; ++j) lo[j] = kf[j] - bf2f((unsigned short)ahi[t][j]);
      alo[t] = pack8(lo);
    }
#pragma unroll
    for (int i = 0; i < 2; ++i) {
      if (w2i + 4 * i < 7) {
        const int nt = 7 * nh + w2i + 4 * i;
        f32x16 m = {};
#pragma unroll
        for (int t = 0; t < 2; ++t) {
          m = MFMA32(ahi[t], wov[i][t], m);
          m = MFMA32(alo[t], wov[i][t], m);
        }
        f32x8 m0, m1;
#pragma unroll
        for (int j = 0; j < 8; ++j) { m0[j] = m[j]; m1[j] = m[j + 8]; }
        const unsigned off = WS_W + (unsigned)(nt * NKS + 112 + 2 * h) * 1024u + lane * 16;
        st16(rs, off, pack8(m0));
        st16(rs, off + 1024, pack8(m1));
      }
    }
    if (nh == 0 && w2i == 3 && lane < 4) {
      const int t = lane >> 1, zh = lane & 1;
      f32x8 z;
#pragma unroll
      for (int j = 0; j < 8; ++j) {
        const int dd = 16 * t + 8 * (j >> 2) + 4 * zh + (j & 3);
        const float zv = kvs[dd * 36 + 28];
        z[j] = (dd < 28) ? zv : 0.f;
      }
      st16(rs, WS_Z + (unsigned)(h * 64 + lane * 16), pack8(z));
    }
    asm volatile("s_waitcnt vmcnt(0)" ::: "memory");
    __syncthreads();
    if (tid2 == 0) stflag(ws + WS_FM + p * 8, tag);
    STAMP(1, 5);
    STAMP_FLUSH(1);
    return;
  }

  const int j7 = bi - 4;
  const int rt = xcd * 4 + j7 / 7, ct = j7 % 7;
  const unsigned Aoff = WS_H + (unsigned)(2 * rt * NKS) * 1024u + lane * 16;
  const unsigned Boff = WS_W + (unsigned)(2 * ct * NKS) * 1024u + lane * 16;
  constexpr unsigned TS = NKS * 1024u;

  const float l0 = __builtin_bit_cast(float, __builtin_amdgcn_raw_buffer_load_b32(rs, WS_G + (unsigned)(rt * 64 + r) * 4u, 0, 16));
  const float l1 = __builtin_bit_cast(float, __builtin_amdgcn_raw_buffer_load_b32(rs, WS_G + (unsigned)(rt * 64 + 32 + r) * 4u, 0, 16));

  f32x16 acc[2][2] = {};
  bf16x8 a0[K2_D], a1[K2_D], b0[K2_D], b1[K2_D], zf[K2_D];
  constexpr int I_A = K2_IA;
  const int rot = 0;
  auto kof = [&](int i) {
    if (i < I_A || i >= I_A + 8) {
      int j = (i < I_A ? i : i - 8) + rot; j = (j >= 28) ? j - 28 : j;
      return (unsigned)(4 * j + w2i) * 1024u;
    }
    return (unsigned)(112 + 8 * w2i + (i - I_A)) * 1024u;
  };
  auto ldA = [&](int i) { a0[i % K2_D] = ld16a(rs, Aoff + kof(i)); a1[i % K2_D] = ld16a(rs, Aoff + TS + kof(i)); };
  auto ldB = [&](int i) {
    b0[i % K2_D] = ld16b(rs, Boff + kof(i)); b1[i % K2_D] = ld16b(rs, Boff + TS + kof(i));
    if (i >= I_A && i < I_A + 8) zf[i % K2_D] = ld16(rs, WS_Z + (unsigned)((8 * w2i + (i - I_A)) * 32 + hh * 16));
  };
#pragma unroll
  for (int i = 0; i < K2_D; ++i) ldB(i);
  __builtin_amdgcn_sched_barrier(0);
  {
    const int fl = ((lane >> 2) < 14 && (lane & 3) < 3) ? lane : 0;
    wait_flags<1>(ws + WS_F1 + (rt * 64 + fl) * 8, 0, 1, 0, tag);
  }
  STAMP(1, 7);
#pragma unroll
  for (int i = 0; i < K2_D; ++i) ldA(i);
  __builtin_amdgcn_sched_barrier(0);
  const float g0 = __expf(l0), g1 = __expf(l1);

  constexpr int I_CHK = I_A - K2_D, I_POLL = I_CHK - K2_D + 1;
  const char* fatt = (lane < 32) ? ws + WS_FM + lane * 8
                                 : ws + WS_F1 + (rt * 64 + (14 + ((lane >> 2) & 1)) * 4 + ((lane & 3) < 3 ? (lane & 3) : 0)) * 8;
  u64 fv = 0;
  static_for<0, 36>([&](auto ic) {
    constexpr int i = decltype(ic)::value;
    if constexpr (i == 1) STAMP(1, 6);
    if constexpr (i == I_POLL) fv = ldflag(fatt);
    constexpr int sl = i % K2_D;
    if constexpr (i < I_A || i >= I_A + 8) {
      acc[0][0] = MFMA32(b0[sl], a0[sl], acc[0][0]);
      acc[0][1] = MFMA32(b1[sl], a0[sl], acc[0][1]);
      acc[1][0] = MFMA32(b0[sl], a1[sl], acc[1][0]);
      acc[1][1] = MFMA32(b1[sl], a1[sl], acc[1][1]);
      if constexpr (i == I_CHK) {
        STAMP(1, 1);
        if (!__all(fv == tag)) {
          unsigned spins = 0;
          u64 f2;
#pragma nounroll
          do { __builtin_amdgcn_s_sleep(4); f2 = ldflag(fatt); ++spins; } while (!__all(f2 == tag) && spins < (1u << 20));
        }
        __builtin_amdgcn_fence(__ATOMIC_ACQUIRE, "wavefront");
        asm volatile("" ::: "memory");
        STAMP(1, 2);
      }
      if constexpr (i + K2_D < 36) { ldA(i + K2_D); ldB(i + K2_D); }
    } else if constexpr (((i - I_A) & 1) == 1) {
      constexpr int s0 = (i - 1) % K2_D, s1 = sl;
#pragma unroll
      for (int st = 0; st < 2; ++st) {
        f32x16 den = {}, p0 = {}, p1 = {};
        den = MFMA32(zf[s0], st ? a1[s0] : a0[s0], den);
        p0 = MFMA32(b0[s0], st ? a1[s0] : a0[s0], p0);
        p1 = MFMA32(b1[s0], st ? a1[s0] : a0[s0], p1);
        den = MFMA32(zf[s1], st ? a1[s1] : a0[s1], den);
        p0 = MFMA32(b0[s1], st ? a1[s1] : a0[s1], p0);
        p1 = MFMA32(b1[s1], st ? a1[s1] : a0[s1], p1);
        const float sc = (st ? g1 : g0) * __builtin_amdgcn_rcpf(den[0] + 1e-6f);
#pragma unroll
        for (int e = 0; e < 16; ++e) { acc[st][0][e] += sc * p0[e]; acc[st][1][e] += sc * p1[e]; }
        asm volatile("" : "+v"(acc[st][0]), "+v"(acc[st][1]));
        __builtin_amdgcn_sched_barrier(0);
      }
      if constexpr (i - 1 + K2_D < 36) { ldA(i - 1 + K2_D); ldB(i - 1 + K2_D); }
      if constexpr (i + K2_D < 36) { ldA(i + K2_D); ldB(i + K2_D); }
      if constexpr (i == I_A + 7) STAMP(1, 3);
    }
    __builtin_amdgcn_sched_barrier(0);
  });

  STAMP(1, 4);
#pragma unroll
  for (int tl = 0; tl < 4; ++tl) {
    if (tl != w2i) {
      const int slot = tl * 3 + (w2i > tl ? w2i - 1 : w2i);
      char* dst = smem + slot * 4096 + lane * 16;
#pragma unroll
      for (int g = 0; g < 4; ++g) {
        const f32x16& a = acc[tl >> 1][tl & 1];
        f32x4 v = {a[4 * g], a[4 * g + 1], a[4 * g + 2], a[4 * g + 3]};
        *(f32x4*)(dst + g * 1024) = v;
      }
    }
  }
  __syncthreads();
  f32x16 fin = (w2i == 0) ? acc[0][0] : ((w2i == 1) ? acc[0][1] : ((w2i == 2) ? acc[1][0] : acc[1][1]));
#pragma unroll
  for (int sI = 0; sI < 3; ++sI) {
    const char* src = smem + (w2i * 3 + sI) * 4096 + lane * 16;
#pragma unroll
    for (int g = 0; g < 4; ++g) {
      const f32x4 v = *(const f32x4*)(src + g * 1024);
      fin[4 * g] += v[0]; fin[4 * g + 1] += v[1]; fin[4 * g + 2] += v[2]; fin[4 * g + 3] += v[3];
    }
  }
  {
    const float omg = __builtin_amdgcn_rcpf(1.f + ((w2i >> 1) ? g1 : g0));
    unsigned short* orow = out + (size_t)(rt * 64 + (w2i >> 1) * 32 + r) * 448 + ct * 64 + (w2i & 1) * 32 + 4 * hh;
#pragma unroll
    for (int g = 0; g < 4; ++g) {
      f32x4 v = {fin[4 * g] * omg, fin[4 * g + 1] * omg, fin[4 * g + 2] * omg, fin[4 * g + 3] * omg};
      *(u32x2*)(orow + 8 * g) = __builtin_bit_cast(u32x2, __builtin_convertvector(v, bf16x4b));
    }
  }
  asm volatile(".Lgm_code_end:");
  STAMP(1, 5);
  STAMP_FLUSH(1);
}

extern "C" void kernel_launch(void* const* d_in, const int* in_sizes, int n_in, void* d_out, int out_size, void* d_ws,
                              size_t ws_size, hipStream_t stream) {
  const unsigned short* x = (const unsigned short*)d_in[0];
  const unsigned short* wqkv = (const unsigned short*)d_in[1];
  const unsigned short* wo = (const unsigned short*)d_in[2];
  const unsigned short* w1 = (const unsigned short*)d_in[4];
  const unsigned short* w3 = (const unsigned short*)d_in[5];
  const unsigned short* w2 = (const unsigned short*)d_in[6];
  const unsigned short* gw = (const unsigned short*)d_in[7];
  (void)hipFuncSetAttribute((const void*)gm_fused, hipFuncAttributeMaxDynamicSharedMemorySize, K1_LDS);
  gm_fused<<<256, 512, K1_LDS, stream>>>(x, wqkv, w1, w3, w2, wo, gw, (char*)d_ws, (unsigned short*)d_out);
}
```

```cpp
#include <hip/hip_runtime.h>
#include <hip/hip_bf16.h>
#include <stdint.h>
#include <type_traits>

typedef __attribute__((ext_vector_type(8))) short bf16x8;
typedef __attribute__((ext_vector_type(4))) short s16x4;
typedef __attribute__((ext_vector_type(8))) __bf16 bf16x8b;
typedef __attribute__((ext_vector_type(4))) __bf16 bf16x4b;
typedef __attribute__((ext_vector_type(8))) float f32x8;
typedef __attribute__((ext_vector_type(4))) float f32x4;
typedef __attribute__((ext_vector_type(16))) float f32x16;
typedef __attribute__((ext_vector_type(4))) unsigned u32x4;
typedef __attribute__((ext_vector_type(2))) unsigned u32x2;
typedef __attribute__((address_space(3))) s16x4* lds_s16x4_ptr;
typedef __attribute__((address_space(3))) unsigned* lds_u32_ptr;
typedef __attribute__((address_space(1))) unsigned gu32;
typedef __attribute__((address_space(1))) unsigned long long gu64;
typedef unsigned long long u64;

#define DI __device__ __forceinline__
#define MFMA32(a, b, c) __builtin_amdgcn_mfma_f32_32x32x16_bf16((a), (b), (c), 0, 0, 0)

constexpr int NKS = 144;
constexpr unsigned WS_H = 0;
constexpr unsigned WS_W = WS_H + 64u * NKS * 1024u;
constexpr unsigned WS_KV = WS_W + 14u * NKS * 1024u;
constexpr unsigned WS_WO = WS_KV + 16u * 16u * 4096u;
constexpr unsigned WS_Z = WS_WO + 448u * 1024u;
constexpr unsigned WS_G = WS_Z + 1024u;
constexpr unsigned WS_EP = WS_G + 8192u;
constexpr unsigned WS_F0 = WS_EP + 256u;
constexpr unsigned WS_F1 = WS_F0 + 2048u;
constexpr unsigned WS_FM = WS_F1 + 16384u;
constexpr unsigned WS_END = WS_FM + 2048u;

#define STAMP(kid, idx) do {} while (0)
#define STAMP_DECL do {} while (0)
#define STAMP_FLUSH(kid) do {} while (0)
#define STAMP_ARG
#define STAMP_PASS
template <int B, int E, class F> DI void static_for(F&& f) {
  if constexpr (B < E) { f(std::integral_constant<int, B>{}); static_for<B + 1, E>(f); }
}
DI bf16x8 pack8(f32x8 v) { return __builtin_bit_cast(bf16x8, __builtin_convertvector(v, bf16x8b)); }
DI float bf2f(unsigned short u) { return __uint_as_float(((unsigned)u) << 16); }
DI float phi_f(float t) { return (fmaxf(t, 0.f) + __expf(fminf(t, 0.f))) + 1e-6f; }
DI void glds16(const char* sbase, unsigned voff, unsigned lds_dst) {
  unsigned keep;
  asm volatile("s_mov_b32 %0, m0\n\ts_mov_b32 m0, %3\n\ts_nop 0\n\tglobal_load_lds_dwordx4 %1, %2\n\ts_mov_b32 m0, %0"
               : "=&s"(keep) : "v"(voff), "s"(sbase), "s"(lds_dst) : "memory");
}

#define RSRC __amdgpu_buffer_rsrc_t
DI void st16(RSRC rs, unsigned off, bf16x8 v) { __builtin_amdgcn_raw_buffer_store_b128(__builtin_bit_cast(u32x4, v), rs, off, 0, 16); }
DI void st16f(RSRC rs, unsigned off, f32x4 v) { __builtin_amdgcn_raw_buffer_store_b128(__builtin_bit_cast(u32x4, v), rs, off, 0, 16); }
DI bf16x8 ld16(RSRC rs, unsigned off) { return __builtin_bit_cast(bf16x8, __builtin_amdgcn_raw_buffer_load_b128(rs, off, 0, 16)); }
#define EXP_AUXA 16
#define EXP_AUXB 16
DI bf16x8 ld16a(RSRC rs, unsigned off) { return __builtin_bit_cast(bf16x8, __builtin_amdgcn_raw_buffer_load_b128(rs, off, 0, EXP_AUXA)); }
DI bf16x8 ld16b(RSRC rs, unsigned off) { return __builtin_bit_cast(bf16x8, __builtin_amdgcn_raw_buffer_load_b128(rs, off, 0, EXP_AUXB)); }
DI f32x4 ld16f(RSRC rs, unsigned off) { return __builtin_bit_cast(f32x4, __builtin_amdgcn_raw_buffer_load_b128(rs, off, 0, 16)); }
DI u64 ldflag(const char* p) { return __hip_atomic_load((gu64*)p, __ATOMIC_RELAXED, __HIP_MEMORY_SCOPE_AGENT); }
DI void stflag(char* p, u64 v) { __hip_atomic_store((gu64*)p, v, __ATOMIC_RELAXED, __HIP_MEMORY_SCOPE_AGENT); }
template <int NL, int SLP = 4> DI void wait_flags(const char* p, int lane, int cnt, int stride, u64 tag) {
#pragma nounroll
  for (unsigned spins = 0; spins < (1u << 20); ++spins) {
    bool ok = true;
#pragma unroll
    for (int i = 0; i < NL; ++i) { const int ix = i * 64 + lane; ok &= (ldflag(p + (ix < cnt ? ix : 0) * stride) == tag); }
    if (__all(ok)) break;
    __builtin_amdgcn_s_sleep(SLP);
  }
  __builtin_amdgcn_fence(__ATOMIC_ACQUIRE, "wavefront");
  asm volatile("" ::: "memory");
}

constexpr int K1_R = 8, K1_U = K1_R + 6, K1_WSLOT = 5 * 2048;
constexpr int K1_XOFF = K1_R * K1_WSLOT;
constexpr int K1_CH = K1_XOFF + K1_U * 4096;
constexpr int K1_GW = K1_CH + 16384;
constexpr int K1_PF = K1_GW + 4096;
constexpr int K1_FLAG = K1_PF + 1024;
constexpr int K1_LDS = K1_FLAG + 16;
#define K1_NPRE 3

template <int N> DI void wait_vm_bar() { asm volatile("s_waitcnt vmcnt(%0)\n\ts_barrier" ::"i"(N) : "memory"); }
DI void bar_only() { asm volatile("s_barrier" ::: "memory"); }
#define K1_LA 5
constexpr int k1_allowed(int WI, int XI, int t) {
  const int needW = t + 1 > 27 ? 27 : t + 1, needX = 4 * (needW >> 2) + 3;
  const int tt = t < 0 ? 0 : t;
  const int lastW = tt - 1 + K1_LA > 27 ? 27 : tt - 1 + K1_LA, lastX = tt - 1 + K1_LA + 3 > 27 ? 27 : tt - 1 + K1_LA + 3;
  return WI * (lastW - needW) + XI * (lastX - needX);
}

DI void glds4v(const char* vptr, unsigned lds_dst) {
  unsigned keep;
  asm volatile("s_mov_b32 %0, m0\n\ts_mov_b32 m0, %2\n\ts_nop 0\n\tglobal_load_lds_dword %1, off\n\ts_mov_b32 m0, %0"
               : "=&s"(keep) : "v"(vptr), "s"(lds_dst) : "memory");
}
DI float wave_sum63(float v) {
#define GM_DPP(ctrl, rm) v += __builtin_bit_cast(float, __builtin_amdgcn_update_dpp(0, __builtin_bit_cast(int, v), ctrl, rm, 0xf, false))
  GM_DPP(0x111, 0xf); GM_DPP(0x112, 0xf); GM_DPP(0x114, 0xf); GM_DPP(0x118, 0xf);
  GM_DPP(0x142, 0xa); GM_DPP(0x143, 0xc);
#undef GM_DPP
  return v;
}
DI float dot8(bf16x8 a, bf16x8 b, float c) {
  typedef __attribute__((ext_vector_type(2))) __bf16 bf16x2b;
  const u32x4 ua = __builtin_bit_cast(u32x4, a), ub = __builtin_bit_cast(u32x4, b);
#pragma unroll
  for (int j = 0; j < 4; ++j) {
    const unsigned pa = ua[j], pb = ub[j];
    c = __builtin_amdgcn_fdot2_f32_bf16(__builtin_bit_cast(bf16x2b, pa), __builtin_bit_cast(bf16x2b, pb), c, false);
  }
  return c;
}

template <int NT, int KIND, int WI, int XI, class SIDE>
DI void k1_main(char* smem, int d, const char* const (&wsrc)[3], const unsigned (&wvo)[3], const unsigned (&wstr)[3],
                const char* xsrc, unsigned xvo, unsigned xvo2, SIDE&& side, const unsigned (&taddr)[NT], unsigned xaddr0, int st0,
                f32x16 (&acc)[2][NT] STAMP_ARG) {
  constexpr int NS = 2, R = K1_R, U = K1_U, WSLOT = K1_WSLOT;
  constexpr bool DMA = WI + XI > 0;
  const unsigned lds0 = (unsigned)(uintptr_t)(__attribute__((address_space(3))) char*)smem;
  const unsigned wdst0 = lds0 + (unsigned)(d * 1024);
  const unsigned xdst0 = lds0 + (unsigned)(K1_XOFF + (d & 1) * 2048);

  auto issueW = [&](int k, int slot) {
#pragma unroll
    for (int m = 0; m < WI; ++m)
      glds16(wsrc[m] + (size_t)((unsigned)k * wstr[m]), wvo[m], (unsigned)(slot * WSLOT) + wdst0 + m * 4096);
  };
  auto issueX = [&](int n, int unit) {
    if constexpr (XI > 0) {
      const char* src = xsrc + (size_t)((unsigned)(n & 3) * (32u * 896u) + (unsigned)(n >> 2) * 128u);
      glds16(src, xvo, xdst0 + (unsigned)(unit * 4096));
      glds16(src, xvo2, xdst0 + (unsigned)(unit * 4096) + 1024);
    }
  };
  auto readX = [&](bf16x8 (&xf)[NS], int st, int unit0, int kk) {
    int un = unit0 + st; un = (un >= U) ? un - U : un;
    xf[st] = *(const bf16x8*)(smem + K1_XOFF + un * 4096 + (xaddr0 ^ (unsigned)(kk << 5)));
  };
  auto readW = [&](bf16x8 (&wf)[NT], int t, int slot) {
    const char* wb = smem + slot * WSLOT;
    s16x4 lo = __builtin_amdgcn_ds_read_tr16_b64_v4i16((lds_s16x4_ptr)(wb + taddr[t]));
    s16x4 hi = __builtin_amdgcn_ds_read_tr16_b64_v4i16((lds_s16x4_ptr)(wb + taddr[t] + 512));
    wf[t] = __builtin_shufflevector(lo, hi, 0, 1, 2, 3, 4, 5, 6, 7);
  };

  if constexpr (DMA) {
#pragma unroll
    for (int e = -3; e <= K1_LA - 1; ++e) {
      issueX(e + 3, e + 3);
      if (e >= 0) issueW(e, e);
    }
    STAMP(0, 4);
    wait_vm_bar<k1_allowed(WI, XI, -1)>();
  } else {
    bar_only();
  }
  STAMP(0, 1);
  bf16x8 xa[NS], wa[NT], xb[NS], wb2[NT];
#pragma unroll
  for (int st = 0; st < NS; ++st) readX(xa, st, st0, 0);
#pragma unroll
  for (int t = 0; t < NT; ++t) readW(wa, t, 0);

  auto step = [&](auto tc, bf16x8 (&xc)[NS], bf16x8 (&wc)[NT], bf16x8 (&xn)[NS], bf16x8 (&wn)[NT]) {
    constexpr int t = decltype(tc)::value;
    if constexpr (DMA) wait_vm_bar<k1_allowed(WI, XI, t)>(); else bar_only();
    constexpr int rslot = (t + 1) % R;
    constexpr int runit = (4 * ((t + 1) >> 2)) % U;
    constexpr int kk = (t + 1) & 3;
    constexpr int NM = NS * NT;
    constexpr int NR = NS + NT;
    constexpr int RPS = (NR + NM - 3) / (NM - 2);
#pragma unroll
    for (int i = 0; i < NM; ++i) {
      const int tt = i / NS, st = i % NS;
      __builtin_amdgcn_sched_barrier(0);
      if constexpr (KIND == 0) acc[st][tt] = MFMA32(wc[tt], xc[st], acc[st][tt]);
      else acc[st][tt] = MFMA32(xc[st], wc[tt], acc[st][tt]);
      __builtin_amdgcn_sched_barrier(0);
      if constexpr (t < 27) {
#pragma unroll
        for (int q = i * RPS; q < (i + 1) * RPS && q < NR; ++q) {
          if (q == 0) readX(xn, 0, runit + st0, kk);
          else if (q == 1) readW(wn, 0, rslot);
          else if (q == 2) readX(xn, 1, runit + st0, kk);
          else readW(wn, q - NS, rslot);
        }
      }
      if constexpr (DMA) {
        if (i == NM - 2) { if constexpr (t + K1_LA + 3 < 28) issueX(t + K1_LA + 3, (t + K1_LA + 3) % U); }
        if (i == NM - 1) { if constexpr (t + K1_LA < 28) issueW(t + K1_LA, (t + K1_LA) % R); }
      } else {
        if (i == NM - 2) side(tc);
      }
    }
    __builtin_amdgcn_sched_barrier(0);
  };
  static_for<0, 14>([&](auto pc) {
    constexpr int t = 2 * decltype(pc)::value;
    step(std::integral_constant<int, t>{}, xa, wa, xb, wb2);
    step(std::integral_constant<int, t + 1>{}, xb, wb2, xa, wa);
  });
  STAMP(0, 2);
}

DI unsigned taddr_ffn(int lane, int i) {
  const int hh = lane >> 5, blk = (lane >> 4) & 1, q = (lane & 15) >> 2, p = lane & 3;
  const int lc = (i >> 2) + 2 * blk, inoff = (i & 3) * 32 + 8 * p;
  const int pos = (inoff >> 4) ^ (4 * ((q >> 1) & 1)) ^ (2 * blk);
  return (unsigned)(lc * 2048 + (8 * hh + q) * 128 + pos * 16 + (inoff & 15));
}
DI unsigned taddr_head(int lane, int lcb, int o) {
  const int hh = lane >> 5, blk = (lane >> 4) & 1, q = (lane & 15) >> 2, p = lane & 3;
  const int pp = (blk == 1 && p == 3) ? 2 : p;
  const int vo = o + 32 * blk + 8 * pp;
  const int lc = lcb + (vo >> 7), inoff = vo & 127;
  const int pos = (inoff >> 4) ^ (4 * ((q >> 1) & 1));
  return (unsigned)(lc * 2048 + (8 * hh + q) * 128 + pos * 16 + (inoff & 15));
}

template <int NS, int NT>
DI void k1_epi_ffn(f32x16 (&acc)[NS][NT], RSRC rs, int lane, int stile_g, int kstep0) {
#pragma unroll
  for (int st = 0; st < NS; ++st)
#pragma unroll
    for (int t = 0; t < NT; ++t) {
      f32x8 hv;
#pragma unroll
      for (int j = 0; j < 8; ++j) {
        const float gt = acc[st][t][j], up = acc[st][t][j + 8];
        hv[j] = gt * __builtin_amdgcn_rcpf(1.f + __expf(-gt)) * up;
      }
      st16(rs, WS_H + (unsigned)((stile_g + st) * NKS + kstep0 + t) * 1024u + lane * 16, pack8(hv));
    }
}

template <int NS, int NT>
DI void k1_epi_q(f32x16 (&acc)[NS][NT], RSRC rs, int lane, int stile_g, int head0) {
#pragma unroll
  for (int st = 0; st < NS; ++st)
#pragma unroll
    for (int t = 0; t < NT; ++t) {
      f32x8 a, b;
#pragma unroll
      for (int j = 0; j < 8; ++j) {
        a[j] = phi_f(acc[st][t][j]);
        b[j] = phi_f(acc[st][t][j + 8]);
      }
      const unsigned dst = WS_H + (unsigned)((stile_g + st) * NKS + 112 + 2 * (head0 + t)) * 1024u + lane * 16;
      st16(rs, dst, pack8(a));
      st16(rs, dst + 1024, pack8(b));
    }
}

DI void k1_epi_kv(f32x16 (&acc)[2][2], RSRC rs, char* smem, int lane, int sh, unsigned dst) {
  const int r = lane & 31;
  f32x16 kv = {};
#pragma unroll
  for (int st = 0; st < 2; ++st)
#pragma unroll
    for (int t = 0; t < 2; ++t) {
      f32x8 pk, vv;
#pragma unroll
      for (int j = 0; j < 8; ++j) {
        pk[j] = phi_f(acc[st][0][8 * t + j]);
        vv[j] = (r == 28) ? 1.f : acc[st][1][8 * t + j];
      }
      kv = MFMA32(pack8(pk), pack8(vv), kv);
    }
  char* xch = smem + K1_CH;
  volatile unsigned* flag = (volatile unsigned*)(smem + K1_FLAG);
  if (sh == 1) {
#pragma unroll
    for (int g = 0; g < 4; ++g) {
      f32x4 v = {kv[4 * g], kv[4 * g + 1], kv[4 * g + 2], kv[4 * g + 3]};
      *(f32x4*)(xch + g * 1024 + lane * 16) = v;
    }
    asm volatile("s_waitcnt lgkmcnt(0)" ::: "memory");
    if (lane == 0) *flag = 1u;
  } else {
#pragma nounroll
    for (unsigned spins = 0; *flag == 0u && spins < (1u << 24); ++spins) __builtin_amdgcn_s_sleep(1);
    asm volatile("" ::: "memory");
#pragma unroll
    for (int g = 0; g < 4; ++g) {
      const f32x4 o = *(const f32x4*)(xch + g * 1024 + lane * 16);
      f32x4 v = {kv[4 * g] + o[0], kv[4 * g + 1] + o[1], kv[4 * g + 2] + o[2], kv[4 * g + 3] + o[3]};
      st16f(rs, dst + g * 1024 + lane * 16, v);
    }
  }
}

constexpr int K2_D = 5;
#define K2_IA 28

__global__ __launch_bounds__(512, 2) void gm_fused(const unsigned short* __restrict__ x, const unsigned short* __restrict__ wqkv,
                                                   const unsigned short* __restrict__ w1, const unsigned short* __restrict__ w3,
                                                   const unsigned short* __restrict__ w2, const unsigned short* __restrict__ wo,
                                                   const unsigned short* __restrict__ gw, char* __restrict__ ws,
                                                   unsigned short* __restrict__ out) {
  extern __shared__ __attribute__((aligned(16))) char smem[];
  const int tid = threadIdx.x, lane = tid & 63;
  const int w = __builtin_amdgcn_readfirstlane(tid >> 6);
  const int b = blockIdx.x, xcd = b & 7, bi = b >> 3;
  const int rg = 8 * (xcd & 1) + (bi & 7);
  const int cq = 4 * (xcd >> 1) + (bi >> 3);
  const int cg = (cq < 8) ? (((cq & 3) == 3) ? 14 + (cq >> 2) : 3 * (cq >> 2) + (cq & 3)) : cq - 2;
  const bool isq = cg >= 14;
  const RSRC rs = __builtin_amdgcn_make_buffer_rsrc(ws, 0, (int)WS_END, 0x00020000);
  const unsigned lds0 = (unsigned)(uintptr_t)(__attribute__((address_space(3))) char*)smem;

  STAMP_DECL;
  STAMP(0, 0);
  const int hh = lane >> 5, r = lane & 31;
  const int d = w & 3;
  const int kvw = (56 * cg) & ~15;
  const int qoff = (cg - 14) * 384;
  const char* xsrc = (const char*)x + (size_t)(rg * 128) * 896;
  const int drow = 8 * (d & 1) + (lane >> 3);
  const int dpos = lane & 7, dsw = 4 * ((lane >> 4) & 1);
  const unsigned xvo = (unsigned)((16 * (d & 1) + (lane >> 3)) * 896 + ((dpos ^ ((lane >> 4) & 7)) << 4));
  const unsigned xvo2 = (unsigned)((16 * (d & 1) + 8 + (lane >> 3)) * 896 + ((dpos ^ ((4 + (lane >> 4)) & 7)) << 4));

  const char* pf = nullptr;
  unsigned pf_step = 0;
  if (w == 0) {
    pf = (const char*)x + (size_t)((xcd & 1) * 1024 + bi * 32 + r) * 896;
    pf_step = 128u;
  } else if (w == 1) {
    const int lc = lane >> 3, kr = (bi & 7) * 8 + (lane & 7);
    const bool ffn = !isq && lc < 4;
    const char* base = ffn ? (const char*)(lc < 2 ? w1 : w3) + 256 * cg + 128 * (lc & 1)
                     : (const char*)wqkv + ((lc < 4) ? qoff + 128 * lc : ((lc < 6) ? 896 : 1792) + kvw + (lc & 1) * 60);
    const unsigned str = ffn ? 3584u : 2688u;
    pf = base + (size_t)kr * str; pf_step = 64u * str;
  }
  if (w < 2) {
#pragma unroll
    for (int c = 0; c < K1_NPRE; ++c) glds4v(pf + (size_t)c * pf_step, lds0 + K1_PF + d * 256);
  }
  if (w == 0) {
    u64 pc; unsigned len;
    asm volatile("s_getpc_b64 %0\n.Lgm_pc_here:\n\ts_mov_b32 %1, .Lgm_code_end-.Lgm_pc_here" : "=s"(pc), "=s"(len));
    const unsigned off = (unsigned)(bi * 16 + (lane & 15)) * 128u;
    glds4v((const char*)pc + ((int)len > 0 && off < len ? off : 0u), lds0 + K1_PF + d * 256);
  }
  const u64 tag = ldflag(ws + WS_EP) + 1ull;

  auto side = [&](auto tc) {
    constexpr int t = decltype(tc)::value;
    const unsigned stg = (unsigned)(K1_CH + d * 8192);
    if constexpr (t % 3 == 1 && K1_NPRE + t / 3 < 7) glds4v(pf + (size_t)(K1_NPRE + t / 3) * pf_step, lds0 + K1_PF + d * 256);
    if constexpr (t == 2 || t == 3 || t == 15 || t == 16) {
      constexpr int i = (t == 2 || t == 15) ? 0 : 1;
      const int slab = b * 4 + d * 2 + i, sl2 = (slab < 1008) ? slab : 0;
      const bool is_wo = sl2 >= 784;
      const int sl_a = is_wo ? (sl2 - 784) / 7 : sl2 / 7;
      const int sl_np = (is_wo ? sl2 - 784 : sl2) - sl_a * 7;
      if constexpr (t < 4) {
        const int row0 = is_wo ? 28 * (sl_a >> 1) + 16 * (sl_a & 1) : 16 * sl_a;
        const int rmax = is_wo ? 447 : 1791;
        const char* src = (const char*)(is_wo ? wo : w2) + sl_np * 128;
        const int r0 = row0 + (lane >> 3), r1 = r0 + 8;
        glds16(src, (unsigned)((r0 < rmax ? r0 : rmax) * 896 + ((dpos ^ dsw) << 4)), lds0 + stg + i * 2048);
        glds16(src, (unsigned)((r1 < rmax ? r1 : rmax) * 896 + ((dpos ^ dsw) << 4)), lds0 + stg + i * 2048 + 1024);
      } else {
        if constexpr (t == 15) asm volatile("s_waitcnt vmcnt(0)" ::: "memory");
        const int blk = (lane >> 4) & 1, q = (lane & 15) >> 2, p = lane & 3;
        const int rlo = (is_wo ? 8 * hh : 4 * hh) + q, rstep = is_wo ? 4 : 8;
#pragma unroll
        for (int nt2 = 0; nt2 < 2; ++nt2) {
          const int inoff = nt2 * 64 + blk * 32 + 8 * p;
          const int pos = (inoff >> 4) ^ (4 * ((q >> 1) & 1));
          const char* src = smem + stg + i * 2048 + rlo * 128 + pos * 16 + (inoff & 15);
          s16x4 lo = __builtin_amdgcn_ds_read_tr16_b64_v4i16((lds_s16x4_ptr)src);
          s16x4 hi = __builtin_amdgcn_ds_read_tr16_b64_v4i16((lds_s16x4_ptr)(src + rstep * 128));
          if (is_wo && (sl_a & 1) && hh) hi = s16x4{0, 0, 0, 0};
          const bf16x8 fr = __builtin_shufflevector(lo, hi, 0, 1, 2, 3, 4, 5, 6, 7);
          const int nt = 2 * sl_np + nt2;
          const unsigned dst = is_wo ? WS_WO + (unsigned)((sl_a >> 1) * 28 + nt * 2 + (sl_a & 1)) * 1024u
                                     : WS_W + (unsigned)(nt * NKS + sl_a) * 1024u;
          if (slab < 1008) st16(rs, dst + lane * 16, fr);
        }
      }
    }
    if constexpr (t == 5 || t == 6 || t == 8) {
      const unsigned gwvo = (unsigned)((lane < 56 ? lane : 55) * 16);
      const char* xr = (const char*)x + (size_t)(b * 8 + d * 4) * 896;
      if constexpr (t == 8) glds16((const char*)gw, gwvo, lds0 + K1_GW + d * 1024);
      else {
        glds16(xr + (t - 5) * 1792, gwvo, lds0 + stg + 4096 + (t - 5) * 2048);
        glds16(xr + (t - 5) * 1792 + 896, gwvo, lds0 + stg + 4096 + (t - 5) * 2048 + 1024);
      }
    }
    if constexpr (t == 17 || t == 18) {
      const bf16x8 gv = *(const bf16x8*)(smem + K1_GW + d * 1024 + lane * 16);
      const bf16x8 xv0 = *(const bf16x8*)(smem + stg + 4096 + (t - 17) * 2048 + lane * 16);
      const bf16x8 xv1 = *(const bf16x8*)(smem + stg + 4096 + (t - 17) * 2048 + 1024 + lane * 16);
      float s0 = dot8(xv0, gv, 0.f), s1 = dot8(xv1, gv, 0.f);
      if (lane >= 56) { s0 = 0.f; s1 = 0.f; }
      s0 = wave_sum63(s0); s1 = wave_sum63(s1);
      if (lane == 63) {
        const u32x2 lg = {__builtin_bit_cast(unsigned, fminf(fmaxf(s0, -60.f), 60.f)), __builtin_bit_cast(unsigned, fminf(fmaxf(s1, -60.f), 60.f))};
        __builtin_amdgcn_raw_buffer_store_b64(lg, rs, WS_G + (unsigned)(b * 8 + d * 4 + (t - 17) * 2) * 4u, 0, 16);
      }
    }
    if constexpr (t == 21) asm volatile("s_waitcnt vmcnt(0)" ::: "memory");
    if constexpr (t == 22) { if (tid == 0) stflag(ws + WS_F0 + b * 8, tag); }
  };
  auto noside = [&](auto) {};

  const unsigned xaddr0 = (unsigned)(r * 128 + ((hh ^ ((r >> 1) & 7)) << 4));
  {
    const char* wsrc[3];
    unsigned wstr[3], wvo[3];
    const unsigned str = isq ? 2688u : 3584u;
    wsrc[0] = isq ? (const char*)wqkv + qoff + 128 * (d >> 1) : (const char*)w1 + 256 * cg + 128 * (d >> 1);
    wsrc[1] = isq ? (const char*)wqkv + qoff + 256 + 128 * (d >> 1) : (const char*)w3 + 256 * cg + 128 * (d >> 1);
    wsrc[2] = (const char*)wqkv + 896 + kvw;
    wvo[0] = (unsigned)drow * str + (unsigned)((dpos ^ dsw) << 4);
    wvo[1] = (unsigned)drow * str + (unsigned)((dpos ^ dsw ^ (isq ? 0 : 2)) << 4);
    { const int c = dpos ^ dsw; wvo[2] = (unsigned)(drow * 2688 + ((c < 4) ? 0 : 896) + (c & 3) * 16); }
    wstr[0] = wstr[1] = 16u * str; wstr[2] = 16u * 2688u;
    const int sh = w & 1, qt = w >> 1;
    const int stile_g = rg * 4 + sh * 2;
    const int qo0 = (cg - 14) * 64;
    if (qt < 2) {
      unsigned ta[3];
#pragma unroll
      for (int t = 0; t < 3; ++t) ta[t] = isq ? taddr_head(lane, 0, qo0 + 56 * (3 * qt + t)) : taddr_ffn(lane, 3 * qt + t);
      f32x16 acc[2][3] = {};
      if (qt == 0) k1_main<3, 0, 0, 0>(smem, d, wsrc, wvo, wstr, xsrc, xvo, xvo2, side, ta, xaddr0, 2 * sh, acc STAMP_PASS);
      else k1_main<3, 0, 0, 2>(smem, d, wsrc, wvo, wstr, xsrc, xvo, xvo2, noside, ta, xaddr0, 2 * sh, acc STAMP_PASS);
      if (isq) k1_epi_q<2, 3>(acc, rs, lane, stile_g, 8 * (cg - 14) + 3 * qt);
      else k1_epi_ffn<2, 3>(acc, rs, lane, stile_g, 8 * cg + 3 * qt);
    } else if (qt == 2) {
      unsigned ta[2];
#pragma unroll
      for (int t = 0; t < 2; ++t) ta[t] = isq ? taddr_head(lane, 0, qo0 + 56 * (6 + t)) : taddr_ffn(lane, 6 + t);
      f32x16 acc[2][2] = {};
      k1_main<2, 0, 3, 0>(smem, d, wsrc, wvo, wstr, xsrc, xvo, xvo2, noside, ta, xaddr0, 2 * sh, acc STAMP_PASS);
      if (isq) k1_epi_q<2, 2>(acc, rs, lane, stile_g, 8 * (cg - 14) + 6);
      else k1_epi_ffn<2, 2>(acc, rs, lane, stile_g, 8 * cg + 6);
    } else {
      unsigned ta[2];
      ta[0] = taddr_head(lane, 4, (56 * cg) & 15);
      ta[1] = taddr_head(lane, 4, 64 + ((56 * cg) & 15));
      f32x16 acc[2][2] = {};
      if (tid == 384) { *(volatile unsigned*)(smem + K1_FLAG) = 0u; *(volatile unsigned*)(smem + K1_FLAG + 4) = 0u; *(volatile unsigned*)(smem + K1_FLAG + 8) = 0u; }
      k1_main<2, 1, 2, 0>(smem, d, wsrc, wvo, wstr, xsrc, xvo, xvo2, noside, ta, xaddr0, 2 * sh, acc STAMP_PASS);
      __builtin_amdgcn_s_setprio(3);
      k1_epi_kv(acc, rs, smem, lane, sh, WS_KV + (unsigned)(rg * 16 + cg) * 4096u);
      __builtin_amdgcn_s_setprio(0);
      if (sh == 1) {
        wait_flags<4>(ws + WS_F0, lane, 256, 8, tag);
        if (lane == 0) *(volatile unsigned*)(smem + K1_FLAG + 8) = 1u;
        if (b == 32 && lane == 0) stflag(ws + WS_EP, tag);
      }
    }
  }
  STAMP(0, 3);
  asm volatile("s_waitcnt vmcnt(0)" ::: "memory");
  if (lane == 0) stflag(ws + WS_F1 + (((rg * 2 + (w & 1)) * 16 + cg) * 4 + (w >> 1)) * 8, tag);
  STAMP_FLUSH(0);
  if (w < 4) {
    if (w == 0 && bi >= 4) {
      const int rt0 = xcd * 4 + (bi - 4) / 7;
      const char* fatt = (lane < 32) ? ws + WS_FM + xcd * 256 + lane * 8
                                     : ws + WS_F1 + (rt0 * 64 + (14 + ((lane >> 2) & 1)) * 4 + ((lane & 3) < 3 ? (lane & 3) : 0)) * 8;
#define EXP_SLP 24
      __builtin_amdgcn_s_sleep(64);
      wait_flags<1, EXP_SLP>(fatt, 0, 1, 0, tag);
      if (lane == 0) *(volatile unsigned*)(smem + K1_FLAG + 4) = 1u;
    }
    return;
  }

  const int w2i = w - 4, tid2 = tid - 256;
  STAMP(1, 0);
#pragma nounroll
  for (unsigned spins = 0; *(volatile unsigned*)(smem + K1_FLAG + 8) == 0u && spins < (1u << 24); ++spins) __builtin_amdgcn_s_sleep(1);
  asm volatile("" ::: "memory");

  if (bi < 4) {
    const int p = xcd * 4 + bi, h = p >> 1, nh = p & 1;
    float* kvs = (float*)smem;
    bf16x8 wov[2][2];
#pragma unroll
    for (int i = 0; i < 2; ++i) {
      const int ntl = (w2i + 4 * i < 7) ? w2i + 4 * i : 0;
#pragma unroll
      for (int t = 0; t < 2; ++t) wov[i][t] = ld16(rs, WS_WO + (unsigned)(h * 28 + (7 * nh + ntl) * 2 + t) * 1024u + lane * 16);
    }
    STAMP(1, 2);
    wait_flags<1>(ws + WS_F1 + (h * 4 + 3) * 8, lane, 16, 1024, tag);
    STAMP(1, 3);
    f32x4 v[16];
#pragma unroll
    for (int g = 0; g < 16; ++g) v[g] = ld16f(rs, WS_KV + (unsigned)(g * 16 + h) * 4096u + tid2 * 16);
    f32x4 acc = {0.f, 0.f, 0.f, 0.f};
#pragma unroll
    for (int g = 0; g < 16; ++g) acc += v[g];
#pragma unroll
    for (int i = 0; i < 4; ++i) kvs[(i + 8 * w2i + 4 * hh) * 36 + r] = acc[i];
    __syncthreads();
    STAMP(1, 1);
    bf16x8 ahi[2], alo[2];
#pragma unroll
    for (int t = 0; t < 2; ++t) {
      const f32x4 k0 = *(const f32x4*)(kvs + r * 36 + 16 * t + 8 * hh), k1 = *(const f32x4*)(kvs + r * 36 + 16 * t + 8 * hh + 4);
      f32x8 kf, lo;
#pragma unroll
      for (int j = 0; j < 8; ++j) {
        const float kvv = (j < 4) ? k0[j & 3] : k1[j & 3];
        kf[j] = (r < 28 && !(t == 1 && hh == 1 && j >= 4)) ? kvv : 0.f;
      }
      ahi[t] = pack8(kf);
#pragma unroll
      for (int j = 0; j < 8; ++j) lo[j] = kf[j] - bf2f((unsigned short)ahi[t][j]);
      alo[t] = pack8(lo);
    }
#pragma unroll
    for (int i = 0; i < 2; ++i) {
      if (w2i + 4 * i < 7) {
        const int nt = 7 * nh + w2i + 4 * i;
        f32x16 m = {};
#pragma unroll
        for (int t = 0; t < 2; ++t) {
          m = MFMA32(ahi[t], wov[i][t], m);
          m = MFMA32(alo[t], wov[i][t], m);
        }
        f32x8 m0, m1;
#pragma unroll
        for (int j = 0; j < 8; ++j) { m0[j] = m[j]; m1[j] = m[j + 8]; }
        const unsigned off = WS_W + (unsigned)(nt * NKS + 112 + 2 * h) * 1024u + lane * 16;
        st16(rs, off, pack8(m0));
        st16(rs, off + 1024, pack8(m1));
      }
    }
    if (nh == 0 && w2i == 3 && lane < 4) {
      const int t = lane >> 1, zh = lane & 1;
      f32x8 z;
#pragma unroll
      for (int j = 0; j < 8; ++j) {
        const int dd = 16 * t + 8 * (j >> 2) + 4 * zh + (j & 3);
        const float zv = kvs[dd * 36 + 28];
        z[j] = (dd < 28) ? zv : 0.f;
      }
      st16(rs, WS_Z + (unsigned)(h * 64 + lane * 16), pack8(z));
    }
    asm volatile("s_waitcnt vmcnt(0)" ::: "memory");
    __syncthreads();
    if (tid2 < 8) stflag(ws + WS_FM + tid2 * 256 + p * 8, tag);
    STAMP(1, 5);
    STAMP_FLUSH(1);
    return;
  }

  const int j7 = bi - 4;
  const int rt = xcd * 4 + j7 / 7, ct = j7 % 7;
  const unsigned Aoff = WS_H + (unsigned)(2 * rt * NKS) * 1024u + lane * 16;
  const unsigned Boff = WS_W + (unsigned)(2 * ct * NKS) * 1024u + lane * 16;
  constexpr unsigned TS = NKS * 1024u;

  const float l0 = __builtin_bit_cast(float, __builtin_amdgcn_raw_buffer_load_b32(rs, WS_G + (unsigned)(rt * 64 + r) * 4u, 0, 16));
  const float l1 = __builtin_bit_cast(float, __builtin_amdgcn_raw_buffer_load_b32(rs, WS_G + (unsigned)(rt * 64 + 32 + r) * 4u, 0, 16));

  f32x16 acc[2][2] = {};
  bf16x8 a0[K2_D], a1[K2_D], b0[K2_D], b1[K2_D], zf[K2_D];
  constexpr int I_A = K2_IA;
  const int rot = 0;
  auto kof = [&](int i) {
    if (i < I_A || i >= I_A + 8) {
      int j = (i < I_A ? i : i - 8) + rot; j = (j >= 28) ? j - 28 : j;
      return (unsigned)(4 * j + w2i) * 1024u;
    }
    return (unsigned)(112 + 8 * w2i + (i - I_A)) * 1024u;
  };
  auto ldA = [&](int i) { a0[i % K2_D] = ld16a(rs, Aoff + kof(i)); a1[i % K2_D] = ld16a(rs, Aoff + TS + kof(i)); };
  auto ldB = [&](int i) {
    b0[i % K2_D] = ld16b(rs, Boff + kof(i)); b1[i % K2_D] = ld16b(rs, Boff + TS + kof(i));
    if (i >= I_A && i < I_A + 8) zf[i % K2_D] = ld16(rs, WS_Z + (unsigned)((8 * w2i + (i - I_A)) * 32 + hh * 16));
  };
#pragma unroll
  for (int i = 0; i < K2_D; ++i) ldB(i);
  __builtin_amdgcn_sched_barrier(0);
  {
    const int fl = ((lane >> 2) < 14 && (lane & 3) < 3) ? lane : 0;
    wait_flags<1>(ws + WS_F1 + (rt * 64 + fl) * 8, 0, 1, 0, tag);
  }
  STAMP(1, 7);
#pragma unroll
  for (int i = 0; i < K2_D; ++i) ldA(i);
  __builtin_amdgcn_sched_barrier(0);
  const float g0 = __expf(l0), g1 = __expf(l1);

  constexpr int I_CHK = I_A - K2_D;
  static_for<0, 36>([&](auto ic) {
    constexpr int i = decltype(ic)::value;
    if constexpr (i == 1) STAMP(1, 6);
    constexpr int sl = i % K2_D;
    if constexpr (i < I_A || i >= I_A + 8) {
      acc[0][0] = MFMA32(b0[sl], a0[sl], acc[0][0]);
      acc[0][1] = MFMA32(b1[sl], a0[sl], acc[0][1]);
      acc[1][0] = MFMA32(b0[sl], a1[sl], acc[1][0]);
      acc[1][1] = MFMA32(b1[sl], a1[sl], acc[1][1]);
      if constexpr (i == I_CHK) {
        STAMP(1, 1);
        if (__builtin_amdgcn_readfirstlane(*(volatile unsigned*)(smem + K1_FLAG + 4)) == 0u) {
#pragma nounroll
          for (unsigned spins = 0; *(volatile unsigned*)(smem + K1_FLAG + 4) == 0u && spins < (1u << 24); ++spins) __builtin_amdgcn_s_sleep(1);
        }
        __builtin_amdgcn_fence(__ATOMIC_ACQUIRE, "wavefront");
        asm volatile("" ::: "memory");
        STAMP(1, 2);
      }
      if constexpr (i + K2_D < 36) { ldA(i + K2_D); ldB(i + K2_D); }
    } else if constexpr (((i - I_A) & 1) == 1) {
      constexpr int s0 = (i - 1) % K2_D, s1 = sl;
#pragma unroll
      for (int st = 0; st < 2; ++st) {
        f32x16 den = {}, p0 = {}, p1 = {};
        den = MFMA32(zf[s0], st ? a1[s0] : a0[s0], den);
        p0 = MFMA32(b0[s0], st ? a1[s0] : a0[s0], p0);
        p1 = MFMA32(b1[s0], st ? a1[s0] : a0[s0], p1);
        den = MFMA32(zf[s1], st ? a1[s1] : a0[s1], den);
        p0 = MFMA32(b0[s1], st ? a1[s1] : a0[s1], p0);
        p1 = MFMA32(b1[s1], st ? a1[s1] : a0[s1], p1);
        const float sc = (st ? g1 : g0) * __builtin_amdgcn_rcpf(den[0] + 1e-6f);
#pragma unroll
        for (int e = 0; e < 16; ++e) { acc[st][0][e] += sc * p0[e]; acc[st][1][e] += sc * p1[e]; }
        asm volatile("" : "+v"(acc[st][0]), "+v"(acc[st][1]));
        __builtin_amdgcn_sched_barrier(0);
      }
      if constexpr (i - 1 + K2_D < 36) { ldA(i - 1 + K2_D); ldB(i - 1 + K2_D); }
      if constexpr (i + K2_D < 36) { ldA(i + K2_D); ldB(i + K2_D); }
      if constexpr (i == I_A + 7) STAMP(1, 3);
    }
    __builtin_amdgcn_sched_barrier(0);
  });

  STAMP(1, 4);
#pragma unroll
  for (int tl = 0; tl < 4; ++tl) {
    if (tl != w2i) {
      const int slot = tl * 3 + (w2i > tl ? w2i - 1 : w2i);
      char* dst = smem + slot * 4096 + lane * 16;
#pragma unroll
      for (int g = 0; g < 4; ++g) {
        const f32x16& a = acc[tl >> 1][tl & 1];
        f32x4 v = {a[4 * g], a[4 * g + 1], a[4 * g + 2], a[4 * g + 3]};
        *(f32x4*)(dst + g * 1024) = v;
      }
    }
  }
  __syncthreads();
  f32x16 fin = (w2i == 0) ? acc[0][0] : ((w2i == 1) ? acc[0][1] : ((w2i == 2) ? acc[1][0] : acc[1][1]));
#pragma unroll
  for (int sI = 0; sI < 3; ++sI) {
    const char* src = smem + (w2i * 3 + sI) * 4096 + lane * 16;
#pragma unroll
    for (int g = 0; g < 4; ++g) {
      const f32x4 v = *(const f32x4*)(src + g * 1024);
      fin[4 * g] += v[0]; fin[4 * g + 1] += v[1]; fin[4 * g + 2] += v[2]; fin[4 * g + 3] += v[3];
    }
  }
  {
    const float omg = __builtin_amdgcn_rcpf(1.f + ((w2i >> 1) ? g1 : g0));
    u32x2 o2[4];
#pragma unroll
    for (int g = 0; g < 4; ++g) {
      f32x4 v = {fin[4 * g] * omg, fin[4 * g + 1] * omg, fin[4 * g + 2] * omg, fin[4 * g + 3] * omg};
      o2[g] = __builtin_bit_cast(u32x2, __builtin_convertvector(v, bf16x4b));
    }
    const RSRC ro = __builtin_amdgcn_make_buffer_rsrc(out, 0, 2048 * 448 * 2, 0x00020000);
    const unsigned obase = (unsigned)((rt * 64 + (w2i >> 1) * 32 + r) * 896 + ct * 128 + (w2i & 1) * 64 + hh * 16);
#pragma unroll
    for (int k = 0; k < 4; k += 2) {
      const auto rx = __builtin_amdgcn_permlane32_swap(o2[k][0], o2[k + 1][0], false, false);
      const auto ry = __builtin_amdgcn_permlane32_swap(o2[k][1], o2[k + 1][1], false, false);
      const u32x4 v = {rx[0], ry[0], rx[1], ry[1]};
#define EXP_OAUX 0
      __builtin_amdgcn_raw_buffer_store_b128(v, ro, obase + 16 * k, 0, EXP_OAUX);
    }
  }
  asm volatile(".Lgm_code_end:");
  STAMP(1, 5);
  STAMP_FLUSH(1);
}

extern "C" void kernel_launch(void* const* d_in, const int* in_sizes, int n_in, void* d_out, int out_size, void* d_ws,
                              size_t ws_size, hipStream_t stream) {
  const unsigned short* x = (const unsigned short*)d_in[0];
  const unsigned short* wqkv = (const unsigned short*)d_in[1];
  const unsigned short* wo = (const unsigned short*)d_in[2];
  const unsigned short* w1 = (const unsigned short*)d_in[4];
  const unsigned short* w3 = (const unsigned short*)d_in[5];
  const unsigned short* w2 = (const unsigned short*)d_in[6];
  const unsigned short* gw = (const unsigned short*)d_in[7];
  (void)hipFuncSetAttribute((const void*)gm_fused, hipFuncAttributeMaxDynamicSharedMemorySize, K1_LDS);
  gm_fused<<<256, 512, K1_LDS, stream>>>(x, wqkv, w1, w3, w2, wo, gw, (char*)d_ws, (unsigned short*)d_out);
}
```

```cpp
#include <hip/hip_runtime.h>
#include <hip/hip_bf16.h>
#include <stdint.h>
#include <type_traits>

typedef __attribute__((ext_vector_type(8))) short bf16x8;
typedef __attribute__((ext_vector_type(4))) short s16x4;
typedef __attribute__((ext_vector_type(8))) __bf16 bf16x8b;
typedef __attribute__((ext_vector_type(4))) __bf16 bf16x4b;
typedef __attribute__((ext_vector_type(8))) float f32x8;
typedef __attribute__((ext_vector_type(4))) float f32x4;
typedef __attribute__((ext_vector_type(16))) float f32x16;
typedef __attribute__((ext_vector_type(4))) unsigned u32x4;
typedef __attribute__((ext_vector_type(2))) unsigned u32x2;
typedef __attribute__((address_space(3))) s16x4* lds_s16x4_ptr;
typedef __attribute__((address_space(1))) unsigned long long gu64;
typedef unsigned long long u64;

#define DI __device__ __forceinline__
#define MFMA32(a, b, c) __builtin_amdgcn_mfma_f32_32x32x16_bf16((a), (b), (c), 0, 0, 0)

constexpr int NKS = 144;
constexpr unsigned WS_H = 0;
constexpr unsigned WS_W = WS_H + 64u * NKS * 1024u;
constexpr unsigned WS_KV = WS_W + 14u * NKS * 1024u;
constexpr unsigned WS_WO = WS_KV + 16u * 16u * 4096u;
constexpr unsigned WS_Z = WS_WO + 448u * 1024u;
constexpr unsigned WS_G = WS_Z + 1024u;
constexpr unsigned WS_EP = WS_G + 8192u;
constexpr unsigned WS_F0 = WS_EP + 256u;
constexpr unsigned WS_F1 = WS_F0 + 2048u;
constexpr unsigned WS_FM = WS_F1 + 16384u;
constexpr unsigned WS_END = WS_FM + 2048u;

template <int B, int E, class F> DI void static_for(F&& f) {
  if constexpr (B < E) { f(std::integral_constant<int, B>{}); static_for<B + 1, E>(f); }
}
DI bf16x8 pack8(f32x8 v) { return __builtin_bit_cast(bf16x8, __builtin_convertvector(v, bf16x8b)); }
DI float bf2f(unsigned short u) { return __uint_as_float(((unsigned)u) << 16); }
DI float phi_f(float t) { return (fmaxf(t, 0.f) + __expf(fminf(t, 0.f))) + 1e-6f; }
DI void glds16(const char* sbase, unsigned voff, unsigned lds_dst) {
  unsigned keep;
  asm volatile("s_mov_b32 %0, m0\n\ts_mov_b32 m0, %3\n\ts_nop 0\n\tglobal_load_lds_dwordx4 %1, %2\n\ts_mov_b32 m0, %0"
               : "=&s"(keep) : "v"(voff), "s"(sbase), "s"(lds_dst) : "memory");
}

#define RSRC __amdgpu_buffer_rsrc_t
DI void st16(RSRC rs, unsigned off, bf16x8 v) { __builtin_amdgcn_raw_buffer_store_b128(__builtin_bit_cast(u32x4, v), rs, off, 0, 16); }
DI void st16f(RSRC rs, unsigned off, f32x4 v) { __builtin_amdgcn_raw_buffer_store_b128(__builtin_bit_cast(u32x4, v), rs, off, 0, 16); }
DI bf16x8 ld16(RSRC rs, unsigned off) { return __builtin_bit_cast(bf16x8, __builtin_amdgcn_raw_buffer_load_b128(rs, off, 0, 16)); }
DI f32x4 ld16f(RSRC rs, unsigned off) { return __builtin_bit_cast(f32x4, __builtin_amdgcn_raw_buffer_load_b128(rs, off, 0, 16)); }
DI u64 ldflag(const char* p) { return __hip_atomic_load((gu64*)p, __ATOMIC_RELAXED, __HIP_MEMORY_SCOPE_AGENT); }
DI void stflag(char* p, u64 v) { __hip_atomic_store((gu64*)p, v, __ATOMIC_RELAXED, __HIP_MEMORY_SCOPE_AGENT); }
template <int NL, int SLP = 4> DI void wait_flags(const char* p, int lane, int cnt, int stride, u64 tag) {
#pragma nounroll
  for (unsigned spins = 0; spins < (1u << 20); ++spins) {
    bool ok = true;
#pragma unroll
    for (int i = 0; i < NL; ++i) { const int ix = i * 64 + lane; ok &= (ldflag(p + (ix < cnt ? ix : 0) * stride) == tag); }
    if (__all(ok)) break;
    __builtin_amdgcn_s_sleep(SLP);
  }
  __builtin_amdgcn_fence(__ATOMIC_ACQUIRE, "wavefront");
  asm volatile("" ::: "memory");
}

constexpr int K1_R = 8, K1_U = K1_R + 6, K1_WSLOT = 5 * 2048;
constexpr int K1_XOFF = K1_R * K1_WSLOT;
constexpr int K1_CH = K1_XOFF + K1_U * 4096;
constexpr int K1_GW = K1_CH + 16384;
constexpr int K1_PF = K1_GW + 4096;
constexpr int K1_FLAG = K1_PF + 1024;
constexpr int K1_LDS = K1_FLAG + 16;
constexpr int K1_NPRE = 3;

template <int N> DI void wait_vm_bar() { asm volatile("s_waitcnt vmcnt(%0)\n\ts_barrier" ::"i"(N) : "memory"); }
DI void bar_only() { asm volatile("s_barrier" ::: "memory"); }
constexpr int K1_LA = 5;
constexpr int k1_allowed(int WI, int XI, int t) {
  const int needW = t + 1 > 27 ? 27 : t + 1, needX = 4 * (needW >> 2) + 3;
  const int tt = t < 0 ? 0 : t;
  const int lastW = tt - 1 + K1_LA > 27 ? 27 : tt - 1 + K1_LA, lastX = tt - 1 + K1_LA + 3 > 27 ? 27 : tt - 1 + K1_LA + 3;
  return WI * (lastW - needW) + XI * (lastX - needX);
}

DI void glds4v(const char* vptr, unsigned lds_dst) {
  unsigned keep;
  asm volatile("s_mov_b32 %0, m0\n\ts_mov_b32 m0, %2\n\ts_nop 0\n\tglobal_load_lds_dword %1, off\n\ts_mov_b32 m0, %0"
               : "=&s"(keep) : "v"(vptr), "s"(lds_dst) : "memory");
}
DI float wave_sum63(float v) {
#define GM_DPP(ctrl, rm) v += __builtin_bit_cast(float, __builtin_amdgcn_update_dpp(0, __builtin_bit_cast(int, v), ctrl, rm, 0xf, false))
  GM_DPP(0x111, 0xf); GM_DPP(0x112, 0xf); GM_DPP(0x114, 0xf); GM_DPP(0x118, 0xf);
  GM_DPP(0x142, 0xa); GM_DPP(0x143, 0xc);
#undef GM_DPP
  return v;
}
DI float dot8(bf16x8 a, bf16x8 b, float c) {
  typedef __attribute__((ext_vector_type(2))) __bf16 bf16x2b;
  const u32x4 ua = __builtin_bit_cast(u32x4, a), ub = __builtin_bit_cast(u32x4, b);
#pragma unroll
  for (int j = 0; j < 4; ++j) {
    const unsigned pa = ua[j], pb = ub[j];
    c = __builtin_amdgcn_fdot2_f32_bf16(__builtin_bit_cast(bf16x2b, pa), __builtin_bit_cast(bf16x2b, pb), c, false);
  }
  return c;
}

template <int NT, int KIND, int WI, int XI, class SIDE>
DI void k1_main(char* smem, int d, const char* const (&wsrc)[3], const unsigned (&wvo)[3], const unsigned (&wstr)[3],
                const char* xsrc, unsigned xvo, unsigned xvo2, SIDE&& side, const unsigned (&taddr)[NT], unsigned xaddr0, int st0,
                f32x16 (&acc)[2][NT]) {
  constexpr int NS = 2, R = K1_R, U = K1_U, WSLOT = K1_WSLOT;
  constexpr bool DMA = WI + XI > 0;
  const unsigned lds0 = (unsigned)(uintptr_t)(__attribute__((address_space(3))) char*)smem;
  const unsigned wdst0 = lds0 + (unsigned)(d * 1024);
  const unsigned xdst0 = lds0 + (unsigned)(K1_XOFF + (d & 1) * 2048);

  auto issueW = [&](int k, int slot) {
#pragma unroll
    for (int m = 0; m < WI; ++m)
      glds16(wsrc[m] + (size_t)((unsigned)k * wstr[m]), wvo[m], (unsigned)(slot * WSLOT) + wdst0 + m * 4096);
  };
  auto issueX = [&](int n, int unit) {
    if constexpr (XI > 0) {
      const char* src = xsrc + (size_t)((unsigned)(n & 3) * (32u * 896u) + (unsigned)(n >> 2) * 128u);
      glds16(src, xvo, xdst0 + (unsigned)(unit * 4096));
      glds16(src, xvo2, xdst0 + (unsigned)(unit * 4096) + 1024);
    }
  };
  auto readX = [&](bf16x8 (&xf)[NS], int st, int unit0, int kk) {
    int un = unit0 + st; un = (un >= U) ? un - U : un;
    xf[st] = *(const bf16x8*)(smem + K1_XOFF + un * 4096 + (xaddr0 ^ (unsigned)(kk << 5)));
  };
  auto readW = [&](bf16x8 (&wf)[NT], int t, int slot) {
    const char* wb = smem + slot * WSLOT;
    s16x4 lo = __builtin_amdgcn_ds_read_tr16_b64_v4i16((lds_s16x4_ptr)(wb + taddr[t]));
    s16x4 hi = __builtin_amdgcn_ds_read_tr16_b64_v4i16((lds_s16x4_ptr)(wb + taddr[t] + 512));
    wf[t] = __builtin_shufflevector(lo, hi, 0, 1, 2, 3, 4, 5, 6, 7);
  };

  if constexpr (DMA) {
#pragma unroll
    for (int e = -3; e <= K1_LA - 1; ++e) {
      issueX(e + 3, e + 3);
      if (e >= 0) issueW(e, e);
    }
    wait_vm_bar<k1_allowed(WI, XI, -1)>();
  } else {
    bar_only();
  }
  bf16x8 xa[NS], wa[NT], xb[NS], wb2[NT];
#pragma unroll
  for (int st = 0; st < NS; ++st) readX(xa, st, st0, 0);
#pragma unroll
  for (int t = 0; t < NT; ++t) readW(wa, t, 0);

  auto step = [&](auto tc, bf16x8 (&xc)[NS], bf16x8 (&wc)[NT], bf16x8 (&xn)[NS], bf16x8 (&wn)[NT]) {
    constexpr int t = decltype(tc)::value;
    if constexpr (DMA) wait_vm_bar<k1_allowed(WI, XI, t)>(); else bar_only();
    constexpr int rslot = (t + 1) % R;
    constexpr int runit = (4 * ((t + 1) >> 2)) % U;
    constexpr int kk = (t + 1) & 3;
    constexpr int NM = NS * NT;
    constexpr int NR = NS + NT;
    constexpr int RPS = (NR + NM - 3) / (NM - 2);
#pragma unroll
    for (int i = 0; i < NM; ++i) {
      const int tt = i / NS, st = i % NS;
      __builtin_amdgcn_sched_barrier(0);
      if constexpr (KIND == 0) acc[st][tt] = MFMA32(wc[tt], xc[st], acc[st][tt]);
      else acc[st][tt] = MFMA32(xc[st], wc[tt], acc[st][tt]);
      __builtin_amdgcn_sched_barrier(0);
      if constexpr (t < 27) {
#pragma unroll
        for (int q = i * RPS; q < (i + 1) * RPS && q < NR; ++q) {
          if (q == 0) readX(xn, 0, runit + st0, kk);
          else if (q == 1) readW(wn, 0, rslot);
          else if (q == 2) readX(xn, 1, runit + st0, kk);
          else readW(wn, q - NS, rslot);
        }
      }
      if constexpr (DMA) {
        if (i == NM - 2) { if constexpr (t + K1_LA + 3 < 28) issueX(t + K1_LA + 3, (t + K1_LA + 3) % U); }
        if (i == NM - 1) { if constexpr (t + K1_LA < 28) issueW(t + K1_LA, (t + K1_LA) % R); }
      } else {
        if (i == NM - 2) side(tc);
      }
    }
    __builtin_amdgcn_sched_barrier(0);
  };
  static_for<0, 14>([&](auto pc) {
    constexpr int t = 2 * decltype(pc)::value;
    step(std::integral_constant<int, t>{}, xa, wa, xb, wb2);
    step(std::integral_constant<int, t + 1>{}, xb, wb2, xa, wa);
  });
}

DI unsigned taddr_ffn(int lane, int i) {
  const int hh = lane >> 5, blk = (lane >> 4) & 1, q = (lane & 15) >> 2, p = lane & 3;
  const int lc = (i >> 2) + 2 * blk, inoff = (i & 3) * 32 + 8 * p;
  const int pos = (inoff >> 4) ^ (4 * ((q >> 1) & 1)) ^ (2 * blk);
  return (unsigned)(lc * 2048 + (8 * hh + q) * 128 + pos * 16 + (inoff & 15));
}
DI unsigned taddr_head(int lane, int lcb, int o) {
  const int hh = lane >> 5, blk = (lane >> 4) & 1, q = (lane & 15) >> 2, p = lane & 3;
  const int pp = (blk == 1 && p == 3) ? 2 : p;
  const int vo = o + 32 * blk + 8 * pp;
  const int lc = lcb + (vo >> 7), inoff = vo & 127;
  const int pos = (inoff >> 4) ^ (4 * ((q >> 1) & 1));
  return (unsigned)(lc * 2048 + (8 * hh + q) * 128 + pos * 16 + (inoff & 15));
}

template <int NS, int NT>
DI void k1_epi_ffn(f32x16 (&acc)[NS][NT], RSRC rs, int lane, int stile_g, int kstep0) {
#pragma unroll
  for (int st = 0; st < NS; ++st)
#pragma unroll
    for (int t = 0; t < NT; ++t) {
      f32x8 hv;
#pragma unroll
      for (int j = 0; j < 8; ++j) {
        const float gt = acc[st][t][j], up = acc[st][t][j + 8];
        hv[j] = gt * __builtin_amdgcn_rcpf(1.f + __expf(-gt)) * up;
      }
      st16(rs, WS_H + (unsigned)((stile_g + st) * NKS + kstep0 + t) * 1024u + lane * 16, pack8(hv));
    }
}

template <int NS, int NT>
DI void k1_epi_q(f32x16 (&acc)[NS][NT], RSRC rs, int lane, int stile_g, int head0) {
#pragma unroll
  for (int st = 0; st < NS; ++st)
#pragma unroll
    for (int t = 0; t < NT; ++t) {
      f32x8 a, b;
#pragma unroll
      for (int j = 0; j < 8; ++j) {
        a[j] = phi_f(acc[st][t][j]);
        b[j] = phi_f(acc[st][t][j + 8]);
      }
      const unsigned dst = WS_H + (unsigned)((stile_g + st) * NKS + 112 + 2 * (head0 + t)) * 1024u + lane * 16;
      st16(rs, dst, pack8(a));
      st16(rs, dst + 1024, pack8(b));
    }
}

DI void k1_epi_kv(f32x16 (&acc)[2][2], RSRC rs, char* smem, int lane, int sh, unsigned dst) {
  const int r = lane & 31;
  f32x16 kv = {};
#pragma unroll
  for (int st = 0; st < 2; ++st)
#pragma unroll
    for (int t = 0; t < 2; ++t) {
      f32x8 pk, vv;
#pragma unroll
      for (int j = 0; j < 8; ++j) {
        pk[j] = phi_f(acc[st][0][8 * t + j]);
        vv[j] = (r == 28) ? 1.f : acc[st][1][8 * t + j];
      }
      kv = MFMA32(pack8(pk), pack8(vv), kv);
    }
  char* xch = smem + K1_CH;
  volatile unsigned* flag = (volatile unsigned*)(smem + K1_FLAG);
  if (sh == 1) {
#pragma unroll
    for (int g = 0; g < 4; ++g) {
      f32x4 v = {kv[4 * g], kv[4 * g + 1], kv[4 * g + 2], kv[4 * g + 3]};
      *(f32x4*)(xch + g * 1024 + lane * 16) = v;
    }
    asm volatile("s_waitcnt lgkmcnt(0)" ::: "memory");
    if (lane == 0) *flag = 1u;
  } else {
#pragma nounroll
    for (unsigned spins = 0; *flag == 0u && spins < (1u << 24); ++spins) __builtin_amdgcn_s_sleep(1);
    asm volatile("" ::: "memory");
#pragma unroll
    for (int g = 0; g < 4; ++g) {
      const f32x4 o = *(const f32x4*)(xch + g * 1024 + lane * 16);
      f32x4 v = {kv[4 * g] + o[0], kv[4 * g + 1] + o[1], kv[4 * g + 2] + o[2], kv[4 * g + 3] + o[3]};
      st16f(rs, dst + g * 1024 + lane * 16, v);
    }
  }
}

constexpr int K2_D = 5;
constexpr int K2_IA = 28;

__global__ __launch_bounds__(512, 2) void gm_fused(const unsigned short* __restrict__ x, const unsigned short* __restrict__ wqkv,
                                                   const unsigned short* __restrict__ w1, const unsigned short* __restrict__ w3,
                                                   const unsigned short* __restrict__ w2, const unsigned short* __restrict__ wo,
                                                   const unsigned short* __restrict__ gw, char* __restrict__ ws,
                                                   unsigned short* __restrict__ out) {
  extern __shared__ __attribute__((aligned(16))) char smem[];
  const int tid = threadIdx.x, lane = tid & 63;
  const int w = __builtin_amdgcn_readfirstlane(tid >> 6);
  const int b = blockIdx.x, xcd = b & 7, bi = b >> 3;
  const int rg = 8 * (xcd & 1) + (bi & 7);
  const int cq = 4 * (xcd >> 1) + (bi >> 3);
  const int cg = (cq < 8) ? (((cq & 3) == 3) ? 14 + (cq >> 2) : 3 * (cq >> 2) + (cq & 3)) : cq - 2;
  const bool isq = cg >= 14;
  const RSRC rs = __builtin_amdgcn_make_buffer_rsrc(ws, 0, (int)WS_END, 0x00020000);
  const unsigned lds0 = (unsigned)(uintptr_t)(__attribute__((address_space(3))) char*)smem;

  const int hh = lane >> 5, r = lane & 31;
  const int d = w & 3;
  const int kvw = (56 * cg) & ~15;
  const int qoff = (cg - 14) * 384;
  const char* xsrc = (const char*)x + (size_t)(rg * 128) * 896;
  const int drow = 8 * (d & 1) + (lane >> 3);
  const int dpos = lane & 7, dsw = 4 * ((lane >> 4) & 1);
  const unsigned xvo = (unsigned)((16 * (d & 1) + (lane >> 3)) * 896 + ((dpos ^ ((lane >> 4) & 7)) << 4));
  const unsigned xvo2 = (unsigned)((16 * (d & 1) + 8 + (lane >> 3)) * 896 + ((dpos ^ ((4 + (lane >> 4)) & 7)) << 4));

  const char* pf = nullptr;
  unsigned pf_step = 0;
  if (w == 0) {
    pf = (const char*)x + (size_t)((xcd & 1) * 1024 + bi * 32 + r) * 896;
    pf_step = 128u;
  } else if (w == 1) {
    const int lc = lane >> 3, kr = (bi & 7) * 8 + (lane & 7);
    const bool ffn = !isq && lc < 4;
    const char* base = ffn ? (const char*)(lc < 2 ? w1 : w3) + 256 * cg + 128 * (lc & 1)
                     : (const char*)wqkv + ((lc < 4) ? qoff + 128 * lc : ((lc < 6) ? 896 : 1792) + kvw + (lc & 1) * 60);
    const unsigned str = ffn ? 3584u : 2688u;
    pf = base + (size_t)kr * str; pf_step = 64u * str;
  }
  if (w < 2) {
#pragma unroll
    for (int c = 0; c < K1_NPRE; ++c) glds4v(pf + (size_t)c * pf_step, lds0 + K1_PF + d * 256);
  }
  if (w == 0) {
    u64 pc; unsigned len;
    asm volatile("s_getpc_b64 %0\n.Lgm_pc_here:\n\ts_mov_b32 %1, .Lgm_code_end-.Lgm_pc_here" : "=s"(pc), "=s"(len));
    const unsigned off = (unsigned)(bi * 16 + (lane & 15)) * 128u;
    glds4v((const char*)pc + ((int)len > 0 && off < len ? off : 0u), lds0 + K1_PF + d * 256);
  }
  const u64 tag = ldflag(ws + WS_EP) + 1ull;

  auto side = [&](auto tc) {
    constexpr int t = decltype(tc)::value;
    constexpr int SJ = 8, SP = 19;
    const unsigned stg = (unsigned)(K1_CH + d * 8192);
    if constexpr (t % 3 == 1 && K1_NPRE + t / 3 < 7) glds4v(pf + (size_t)(K1_NPRE + t / 3) * pf_step, lds0 + K1_PF + d * 256);
    if constexpr (t == SJ || t == SJ + 1 || t == SP || t == SP + 1) {
      constexpr int i = (t == SJ || t == SP) ? 0 : 1;
      const int slab = b * 4 + d * 2 + i, sl2 = (slab < 1008) ? slab : 0;
      const bool is_wo = sl2 >= 784;
      const int sl_a = is_wo ? (sl2 - 784) / 7 : sl2 / 7;
      const int sl_np = (is_wo ? sl2 - 784 : sl2) - sl_a * 7;
      if constexpr (t < SP) {
        const int row0 = is_wo ? 28 * (sl_a >> 1) + 16 * (sl_a & 1) : 16 * sl_a;
        const int rmax = is_wo ? 447 : 1791;
        const char* src = (const char*)(is_wo ? wo : w2) + sl_np * 128;
        const int r0 = row0 + (lane >> 3), r1 = r0 + 8;
        glds16(src, (unsigned)((r0 < rmax ? r0 : rmax) * 896 + ((dpos ^ dsw) << 4)), lds0 + stg + i * 2048);
        glds16(src, (unsigned)((r1 < rmax ? r1 : rmax) * 896 + ((dpos ^ dsw) << 4)), lds0 + stg + i * 2048 + 1024);
      } else {
        if constexpr (t == SP) asm volatile("s_waitcnt vmcnt(0)" ::: "memory");
        const int blk = (lane >> 4) & 1, q = (lane & 15) >> 2, p = lane & 3;
        const int rlo = (is_wo ? 8 * hh : 4 * hh) + q, rstep = is_wo ? 4 : 8;
#pragma unroll
        for (int nt2 = 0; nt2 < 2; ++nt2) {
          const int inoff = nt2 * 64 + blk * 32 + 8 * p;
          const int pos = (inoff >> 4) ^ (4 * ((q >> 1) & 1));
          const char* src = smem + stg + i * 2048 + rlo * 128 + pos * 16 + (inoff & 15);
          s16x4 lo = __builtin_amdgcn_ds_read_tr16_b64_v4i16((lds_s16x4_ptr)src);
          s16x4 hi = __builtin_amdgcn_ds_read_tr16_b64_v4i16((lds_s16x4_ptr)(src + rstep * 128));
          if (is_wo && (sl_a & 1) && hh) hi = s16x4{0, 0, 0, 0};
          const bf16x8 fr = __builtin_shufflevector(lo, hi, 0, 1, 2, 3, 4, 5, 6, 7);
          const int nt = 2 * sl_np + nt2;
          const unsigned dst = is_wo ? WS_WO + (unsigned)((sl_a >> 1) * 28 + nt * 2 + (sl_a & 1)) * 1024u
                                     : WS_W + (unsigned)(nt * NKS + sl_a) * 1024u;
          if (slab < 1008) st16(rs, dst + lane * 16, fr);
        }
      }
    }
    if constexpr (t == SJ + 2 || t == SJ + 3 || t == SJ + 4) {
      const unsigned gwvo = (unsigned)((lane < 56 ? lane : 55) * 16);
      const char* xr = (const char*)x + (size_t)(b * 8 + d * 4) * 896;
      if constexpr (t == SJ + 4) glds16((const char*)gw, gwvo, lds0 + K1_GW + d * 1024);
      else {
        glds16(xr + (t - SJ - 2) * 1792, gwvo, lds0 + stg + 4096 + (t - SJ - 2) * 2048);
        glds16(xr + (t - SJ - 2) * 1792 + 896, gwvo, lds0 + stg + 4096 + (t - SJ - 2) * 2048 + 1024);
      }
    }
    if constexpr (t == SP + 2 || t == SP + 3) {
      const bf16x8 gv = *(const bf16x8*)(smem + K1_GW + d * 1024 + lane * 16);
      const bf16x8 xv0 = *(const bf16x8*)(smem + stg + 4096 + (t - SP - 2) * 2048 + lane * 16);
      const bf16x8 xv1 = *(const bf16x8*)(smem + stg + 4096 + (t - SP - 2) * 2048 + 1024 + lane * 16);
      float s0 = dot8(xv0, gv, 0.f), s1 = dot8(xv1, gv, 0.f);
      if (lane >= 56) { s0 = 0.f; s1 = 0.f; }
      s0 = wave_sum63(s0); s1 = wave_sum63(s1);
      if (lane == 63) {
        const u32x2 lg = {__builtin_bit_cast(unsigned, s0), __builtin_bit_cast(unsigned, s1)};
        __builtin_amdgcn_raw_buffer_store_b64(lg, rs, WS_G + (unsigned)(b * 8 + d * 4 + (t - SP - 2) * 2) * 4u, 0, 16);
      }
    }
    if constexpr (t == SP + 5) asm volatile("s_waitcnt vmcnt(0)" ::: "memory");
    if constexpr (t == SP + 6) { if (tid == 0) stflag(ws + WS_F0 + b * 8, tag); }
  };
  auto noside = [&](auto) {};

  const unsigned xaddr0 = (unsigned)(r * 128 + ((hh ^ ((r >> 1) & 7)) << 4));
  {
    const char* wsrc[3];
    unsigned wstr[3], wvo[3];
    const unsigned str = isq ? 2688u : 3584u;
    wsrc[0] = isq ? (const char*)wqkv + qoff + 128 * (d >> 1) : (const char*)w1 + 256 * cg + 128 * (d >> 1);
    wsrc[1] = isq ? (const char*)wqkv + qoff + 256 + 128 * (d >> 1) : (const char*)w3 + 256 * cg + 128 * (d >> 1);
    wsrc[2] = (const char*)wqkv + 896 + kvw;
    wvo[0] = (unsigned)drow * str + (unsigned)((dpos ^ dsw) << 4);
    wvo[1] = (unsigned)drow * str + (unsigned)((dpos ^ dsw ^ (isq ? 0 : 2)) << 4);
    { const int c = dpos ^ dsw; wvo[2] = (unsigned)(drow * 2688 + ((c < 4) ? 0 : 896) + (c & 3) * 16); }
    wstr[0] = wstr[1] = 16u * str; wstr[2] = 16u * 2688u;
    const int sh = w & 1, qt = w >> 1;
    const int stile_g = rg * 4 + sh * 2;
    const int qo0 = (cg - 14) * 64;
    if (qt < 2) {
      unsigned ta[3];
#pragma unroll
      for (int t = 0; t < 3; ++t) ta[t] = isq ? taddr_head(lane, 0, qo0 + 56 * (3 * qt + t)) : taddr_ffn(lane, 3 * qt + t);
      f32x16 acc[2][3] = {};
      if (qt == 0) k1_main<3, 0, 0, 0>(smem, d, wsrc, wvo, wstr, xsrc, xvo, xvo2, side, ta, xaddr0, 2 * sh, acc);
      else k1_main<3, 0, 0, 2>(smem, d, wsrc, wvo, wstr, xsrc, xvo, xvo2, noside, ta, xaddr0, 2 * sh, acc);
      if (isq) k1_epi_q<2, 3>(acc, rs, lane, stile_g, 8 * (cg - 14) + 3 * qt);
      else k1_epi_ffn<2, 3>(acc, rs, lane, stile_g, 8 * cg + 3 * qt);
    } else if (qt == 2) {
      unsigned ta[2];
#pragma unroll
      for (int t = 0; t < 2; ++t) ta[t] = isq ? taddr_head(lane, 0, qo0 + 56 * (6 + t)) : taddr_ffn(lane, 6 + t);
      f32x16 acc[2][2] = {};
      k1_main<2, 0, 3, 0>(smem, d, wsrc, wvo, wstr, xsrc, xvo, xvo2, noside, ta, xaddr0, 2 * sh, acc);
      if (isq) k1_epi_q<2, 2>(acc, rs, lane, stile_g, 8 * (cg - 14) + 6);
      else k1_epi_ffn<2, 2>(acc, rs, lane, stile_g, 8 * cg + 6);
    } else {
      unsigned ta[2];
      ta[0] = taddr_head(lane, 4, (56 * cg) & 15);
      ta[1] = taddr_head(lane, 4, 64 + ((56 * cg) & 15));
      f32x16 acc[2][2] = {};
      if (tid == 384) { *(volatile unsigned*)(smem + K1_FLAG) = 0u; *(volatile unsigned*)(smem + K1_FLAG + 4) = 0u; *(volatile unsigned*)(smem + K1_FLAG + 8) = 0u; }
      k1_main<2, 1, 2, 0>(smem, d, wsrc, wvo, wstr, xsrc, xvo, xvo2, noside, ta, xaddr0, 2 * sh, acc);
      __builtin_amdgcn_s_setprio(3);
      k1_epi_kv(acc, rs, smem, lane, sh, WS_KV + (unsigned)(rg * 16 + cg) * 4096u);
      __builtin_amdgcn_s_setprio(0);
      if (sh == 1) {
        wait_flags<4>(ws + WS_F0, lane, 256, 8, tag);
        if (lane == 0) *(volatile unsigned*)(smem + K1_FLAG + 8) = 1u;
        if (b == 32 && lane == 0) stflag(ws + WS_EP, tag);
      }
    }
  }
  asm volatile("s_waitcnt vmcnt(0)" ::: "memory");
  if (lane == 0) stflag(ws + WS_F1 + (((rg * 2 + (w & 1)) * 16 + cg) * 4 + (w >> 1)) * 8, tag);
  if (w < 4) {
    if (w == 0 && bi >= 4) {
      const int rt0 = xcd * 4 + (bi - 4) / 7;
      const char* fatt = (lane < 32) ? ws + WS_FM + xcd * 256 + lane * 8
                                     : ws + WS_F1 + (rt0 * 64 + (14 + ((lane >> 2) & 1)) * 4 + ((lane & 3) < 3 ? (lane & 3) : 0)) * 8;
      __builtin_amdgcn_s_sleep(64);
      wait_flags<1, 24>(fatt, 0, 1, 0, tag);
      if (lane == 0) *(volatile unsigned*)(smem + K1_FLAG + 4) = 1u;
    }
    return;
  }

  const int w2i = w - 4, tid2 = tid - 256;
#pragma nounroll
  for (unsigned spins = 0; *(volatile unsigned*)(smem + K1_FLAG + 8) == 0u && spins < (1u << 24); ++spins) __builtin_amdgcn_s_sleep(1);
  asm volatile("" ::: "memory");

  if (bi < 4) {
    const int p = xcd * 4 + bi, h = p >> 1, nh = p & 1;
    float* kvs = (float*)smem;
    bf16x8 wov[2][2];
#pragma unroll
    for (int i = 0; i < 2; ++i) {
      const int ntl = (w2i + 4 * i < 7) ? w2i + 4 * i : 0;
#pragma unroll
      for (int t = 0; t < 2; ++t) wov[i][t] = ld16(rs, WS_WO + (unsigned)(h * 28 + (7 * nh + ntl) * 2 + t) * 1024u + lane * 16);
    }
    wait_flags<1>(ws + WS_F1 + (h * 4 + 3) * 8, lane, 16, 1024, tag);
    f32x4 v[16];
#pragma unroll
    for (int g = 0; g < 16; ++g) v[g] = ld16f(rs, WS_KV + (unsigned)(g * 16 + h) * 4096u + tid2 * 16);
    f32x4 acc = {0.f, 0.f, 0.f, 0.f};
#pragma unroll
    for (int g = 0; g < 16; ++g) acc += v[g];
#pragma unroll
    for (int i = 0; i < 4; ++i) kvs[(i + 8 * w2i + 4 * hh) * 36 + r] = acc[i];
    __syncthreads();
    bf16x8 ahi[2], alo[2];
#pragma unroll
    for (int t = 0; t < 2; ++t) {
      const f32x4 k0 = *(const f32x4*)(kvs + r * 36 + 16 * t + 8 * hh), k1 = *(const f32x4*)(kvs + r * 36 + 16 * t + 8 * hh + 4);
      f32x8 kf, lo;
#pragma unroll
      for (int j = 0; j < 8; ++j) {
        const float kvv = (j < 4) ? k0[j & 3] : k1[j & 3];
        kf[j] = (r < 28 && !(t == 1 && hh == 1 && j >= 4)) ? kvv : 0.f;
      }
      ahi[t] = pack8(kf);
#pragma unroll
      for (int j = 0; j < 8; ++j) lo[j] = kf[j] - bf2f((unsigned short)ahi[t][j]);
      alo[t] = pack8(lo);
    }
#pragma unroll
    for (int i = 0; i < 2; ++i) {
      if (w2i + 4 * i < 7) {
        const int nt = 7 * nh + w2i + 4 * i;
        f32x16 m = {};
#pragma unroll
        for (int t = 0; t < 2; ++t) {
          m = MFMA32(ahi[t], wov[i][t], m);
          m = MFMA32(alo[t], wov[i][t], m);
        }
        f32x8 m0, m1;
#pragma unroll
        for (int j = 0; j < 8; ++j) { m0[j] = m[j]; m1[j] = m[j + 8]; }
        const unsigned off = WS_W + (unsigned)(nt * NKS + 112 + 2 * h) * 1024u + lane * 16;
        st16(rs, off, pack8(m0));
        st16(rs, off + 1024, pack8(m1));
      }
    }
    if (nh == 0 && w2i == 3 && lane < 4) {
      const int t = lane >> 1, zh = lane & 1;
      f32x8 z;
#pragma unroll
      for (int j = 0; j < 8; ++j) {
        const int dd = 16 * t + 8 * (j >> 2) + 4 * zh + (j & 3);
        const float zv = kvs[dd * 36 + 28];
        z[j] = (dd < 28) ? zv : 0.f;
      }
      st16(rs, WS_Z + (unsigned)(h * 64 + lane * 16), pack8(z));
    }
    asm volatile("s_waitcnt vmcnt(0)" ::: "memory");
    __syncthreads();
    if (tid2 < 8) stflag(ws + WS_FM + tid2 * 256 + p * 8, tag);
    return;
  }

  const int j7 = bi - 4;
  const int rt = xcd * 4 + j7 / 7, ct = j7 % 7;
  const unsigned Aoff = WS_H + (unsigned)(2 * rt * NKS) * 1024u + lane * 16;
  const unsigned Boff = WS_W + (unsigned)(2 * ct * NKS) * 1024u + lane * 16;
  constexpr unsigned TS = NKS * 1024u;

  const float l0 = __builtin_bit_cast(float, __builtin_amdgcn_raw_buffer_load_b32(rs, WS_G + (unsigned)(rt * 64 + r) * 4u, 0, 16));
  const float l1 = __builtin_bit_cast(float, __builtin_amdgcn_raw_buffer_load_b32(rs, WS_G + (unsigned)(rt * 64 + 32 + r) * 4u, 0, 16));

  f32x16 acc[2][2] = {};
  bf16x8 a0[K2_D], a1[K2_D], b0[K2_D], b1[K2_D], zf[K2_D];
  constexpr int I_A = K2_IA;
  auto kof = [&](int i) {
    if (i < I_A || i >= I_A + 8) {
      return (unsigned)(4 * (i < I_A ? i : i - 8) + w2i) * 1024u;
    }
    return (unsigned)(112 + 8 * w2i + (i - I_A)) * 1024u;
  };
  auto ldA = [&](int i) { a0[i % K2_D] = ld16(rs, Aoff + kof(i)); a1[i % K2_D] = ld16(rs, Aoff + TS + kof(i)); };
  auto ldB = [&](int i) {
    b0[i % K2_D] = ld16(rs, Boff + kof(i)); b1[i % K2_D] = ld16(rs, Boff + TS + kof(i));
    if (i >= I_A && i < I_A + 8) zf[i % K2_D] = ld16(rs, WS_Z + (unsigned)((8 * w2i + (i - I_A)) * 32 + hh * 16));
  };
#pragma unroll
  for (int i = 0; i < K2_D; ++i) ldB(i);
  __builtin_amdgcn_sched_barrier(0);
  {
    const int fl = ((lane >> 2) < 14 && (lane & 3) < 3) ? lane : 0;
    wait_flags<1>(ws + WS_F1 + (rt * 64 + fl) * 8, 0, 1, 0, tag);
  }
#pragma unroll
  for (int i = 0; i < K2_D; ++i) ldA(i);
  __builtin_amdgcn_sched_barrier(0);
  const float g0 = __builtin_amdgcn_rcpf(1.f + __expf(-l0)), g1 = __builtin_amdgcn_rcpf(1.f + __expf(-l1));

  constexpr int I_CHK = I_A - K2_D;
  static_for<0, 36>([&](auto ic) {
    constexpr int i = decltype(ic)::value;
    constexpr int sl = i % K2_D;
    if constexpr (i < I_A || i >= I_A + 8) {
      acc[0][0] = MFMA32(b0[sl], a0[sl], acc[0][0]);
      acc[0][1] = MFMA32(b1[sl], a0[sl], acc[0][1]);
      acc[1][0] = MFMA32(b0[sl], a1[sl], acc[1][0]);
      acc[1][1] = MFMA32(b1[sl], a1[sl], acc[1][1]);
      if constexpr (i == I_CHK) {
        if (__builtin_amdgcn_readfirstlane(*(volatile unsigned*)(smem + K1_FLAG + 4)) == 0u) {
#pragma nounroll
          for (unsigned spins = 0; *(volatile unsigned*)(smem + K1_FLAG + 4) == 0u && spins < (1u << 24); ++spins) __builtin_amdgcn_s_sleep(1);
        }
        __builtin_amdgcn_fence(__ATOMIC_ACQUIRE, "wavefront");
        asm volatile("" ::: "memory");
      }
      if constexpr (i + K2_D < 36) { ldA(i + K2_D); ldB(i + K2_D); }
      if constexpr (i == I_A - 1) {
        const float f0 = 1.f - g0, f1 = 1.f - g1;
#pragma unroll
        for (int e = 0; e < 16; ++e) { acc[0][0][e] *= f0; acc[0][1][e] *= f0; acc[1][0][e] *= f1; acc[1][1][e] *= f1; }
        asm volatile("" : "+v"(acc[0][0]), "+v"(acc[0][1]), "+v"(acc[1][0]), "+v"(acc[1][1]));
      }
    } else if constexpr (((i - I_A) & 1) == 1) {
      constexpr int s0 = (i - 1) % K2_D, s1 = sl;
#pragma unroll
      for (int st = 0; st < 2; ++st) {
        f32x16 den = {}, p0 = {}, p1 = {};
        den = MFMA32(zf[s0], st ? a1[s0] : a0[s0], den);
        p0 = MFMA32(b0[s0], st ? a1[s0] : a0[s0], p0);
        p1 = MFMA32(b1[s0], st ? a1[s0] : a0[s0], p1);
        den = MFMA32(zf[s1], st ? a1[s1] : a0[s1], den);
        p0 = MFMA32(b0[s1], st ? a1[s1] : a0[s1], p0);
        p1 = MFMA32(b1[s1], st ? a1[s1] : a0[s1], p1);
        const float sc = (st ? g1 : g0) * __builtin_amdgcn_rcpf(den[0] + 1e-6f);
#pragma unroll
        for (int e = 0; e < 16; ++e) { acc[st][0][e] += sc * p0[e]; acc[st][1][e] += sc * p1[e]; }
        asm volatile("" : "+v"(acc[st][0]), "+v"(acc[st][1]));
        __builtin_amdgcn_sched_barrier(0);
      }
      if constexpr (i - 1 + K2_D < 36) { ldA(i - 1 + K2_D); ldB(i - 1 + K2_D); }
      if constexpr (i + K2_D < 36) { ldA(i + K2_D); ldB(i + K2_D); }
    }
    __builtin_amdgcn_sched_barrier(0);
  });

#pragma unroll
  for (int tl = 0; tl < 4; ++tl) {
    if (tl != w2i) {
      const int slot = tl * 3 + (w2i > tl ? w2i - 1 : w2i);
      char* dst = smem + slot * 4096 + lane * 16;
#pragma unroll
      for (int g = 0; g < 4; ++g) {
        const f32x16& a = acc[tl >> 1][tl & 1];
        f32x4 v = {a[4 * g], a[4 * g + 1], a[4 * g + 2], a[4 * g + 3]};
        *(f32x4*)(dst + g * 1024) = v;
      }
    }
  }
  __syncthreads();
  f32x16 fin = (w2i == 0) ? acc[0][0] : ((w2i == 1) ? acc[0][1] : ((w2i == 2) ? acc[1][0] : acc[1][1]));
#pragma unroll
  for (int sI = 0; sI < 3; ++sI) {
    const char* src = smem + (w2i * 3 + sI) * 4096 + lane * 16;
#pragma unroll
    for (int g = 0; g < 4; ++g) {
      const f32x4 v = *(const f32x4*)(src + g * 1024);
      fin[4 * g] += v[0]; fin[4 * g + 1] += v[1]; fin[4 * g + 2] += v[2]; fin[4 * g + 3] += v[3];
    }
  }
  {
    u32x2 o2[4];
#pragma unroll
    for (int g = 0; g < 4; ++g) {
      f32x4 v = {fin[4 * g], fin[4 * g + 1], fin[4 * g + 2], fin[4 * g + 3]};
      o2[g] = __builtin_bit_cast(u32x2, __builtin_convertvector(v, bf16x4b));
    }
    const RSRC ro = __builtin_amdgcn_make_buffer_rsrc(out, 0, 2048 * 448 * 2, 0x00020000);
    const unsigned obase = (unsigned)((rt * 64 + (w2i >> 1) * 32 + r) * 896 + ct * 128 + (w2i & 1) * 64 + hh * 16);
#pragma unroll
    for (int k = 0; k < 4; k += 2) {
      const auto rx = __builtin_amdgcn_permlane32_swap(o2[k][0], o2[k + 1][0], false, false);
      const auto ry = __builtin_amdgcn_permlane32_swap(o2[k][1], o2[k + 1][1], false, false);
      const u32x4 v = {rx[0], ry[0], rx[1], ry[1]};
      __builtin_amdgcn_raw_buffer_store_b128(v, ro, obase + 16 * k, 0, 0);
    }
  }
  asm volatile(".Lgm_code_end:");
}

extern "C" void kernel_launch(void* const* d_in, const int* in_sizes, int n_in, void* d_out, int out_size, void* d_ws,
                              size_t ws_size, hipStream_t stream) {
  const unsigned short* x = (const unsigned short*)d_in[0];
  const unsigned short* wqkv = (const unsigned short*)d_in[1];
  const unsigned short* wo = (const unsigned short*)d_in[2];
  const unsigned short* w1 = (const unsigned short*)d_in[4];
  const unsigned short* w3 = (const unsigned short*)d_in[5];
  const unsigned short* w2 = (const unsigned short*)d_in[6];
  const unsigned short* gw = (const unsigned short*)d_in[7];
  (void)hipFuncSetAttribute((const void*)gm_fused, hipFuncAttributeMaxDynamicSharedMemorySize, K1_LDS);
  gm_fused<<<256, 512, K1_LDS, stream>>>(x, wqkv, w1, w3, w2, wo, gw, (char*)d_ws, (unsigned short*)d_out);
}
```
